```python
import torch

def _softmax(x, axis=-1):
    return torch.softmax(x, dim=axis)
def _gelu(x):
    return torch.nn.functional.gelu(x, approximate="tanh")
def _silu(x):
    return torch.nn.functional.silu(x)
def _sigmoid(x):
    return torch.sigmoid(x)
def _layernorm(x, axis=-1, eps=1e-5):
    m = x.mean(dim=axis, keepdim=True)
    v = x.var(dim=axis, keepdim=True, unbiased=False)
    return (x - m) / torch.sqrt(v + eps)
def _scatter_add(tgt, idx, src, axis=0):
    return tgt.clone().index_add_(axis, idx, src)
def _segment_sum(src, seg, n):
    out = torch.zeros((n,) + src.shape[1:], dtype=src.dtype)
    return out.index_add_(0, seg, src)
def _pad(x, pw):
    flat = tuple(v for pair in reversed(pw) for v in pair)
    return torch.nn.functional.pad(x, flat)

kernel_name = 'gated_mix__compressed_kv_cla__dense_activation_squared_relu__decode'

def setup_inputs():

    _w = (lambda a: (lambda t: t.to(torch.bfloat16) if t.is_floating_point() else t)(torch.as_tensor(a)))
    def _A_setup():
        B, H, Sq, Sk, D, M = 2, 32, 1, 8192, 128, 576
        Dc = 4 * D
        return {
            "x": _w((lambda *s: torch.randn(*s, dtype=torch.bfloat16) * 0.05)(B, Sq, M)),
            "c_kv": _w((lambda *s: torch.randn(*s, dtype=torch.bfloat16) * 1.6)(B, Sk, Dc)),
            "w_q": _w((lambda *s: torch.randn(*s, dtype=torch.bfloat16) * 6.4)(M, H, D) * (M ** -0.5)),
            "w_uk": _w((lambda *s: torch.randn(*s, dtype=torch.bfloat16) * 6.4)(Dc, H, D) * (Dc ** -0.5)),
            "w_uv": _w((lambda *s: torch.randn(*s, dtype=torch.bfloat16) * 1.6)(Dc, H, D) * (Dc ** -0.5)),
            "w_o": _w((lambda *s: torch.randn(*s, dtype=torch.bfloat16) * 1.6)(H, D, M) * ((H * D) ** -0.5)),
        }
    _A_in = _A_setup()

    def _B_setup():
        B, S, D, F = 2, 1, 576, 4608
        return {
            "x": _w((lambda *s: torch.randn(*s, dtype=torch.bfloat16) * 0.05)(B, S, D)),
            "w1": _w((lambda *s: torch.randn(*s, dtype=torch.bfloat16) * 0.4)(D, F)),
            "b1": _w((lambda *s: torch.randn(*s, dtype=torch.bfloat16) * 0.2)(F)),
            "w2": _w((lambda *s: torch.randn(*s, dtype=torch.bfloat16) * 0.2)(F, D)),
            "b2": _w((lambda *s: torch.randn(*s, dtype=torch.bfloat16) * 0.2)(D)),
        }
    _B_in = _B_setup()

    return {
        "A_x": _A_in["x"],
        "A_c_kv": _A_in["c_kv"],
        "A_w_q": _A_in["w_q"],
        "A_w_uk": _A_in["w_uk"],
        "A_w_uv": _A_in["w_uv"],
        "A_w_o": _A_in["w_o"],
        "B_x": _B_in["x"],
        "B_w1": _B_in["w1"],
        "B_b1": _B_in["b1"],
        "B_w2": _B_in["w2"],
        "B_b2": _B_in["b2"],
        "gate_w": _w((lambda *s: torch.randn(*s, dtype=torch.bfloat16) * 0.05)(576) * 16.0),
    }

def reference(A_x, A_c_kv, A_w_q, A_w_uk, A_w_uv, A_w_o, B_x, B_w1, B_b1, B_w2, B_b2, gate_w):
    def _A_ref(x, c_kv, w_q, w_uk, w_uv, w_o):
        B, Sq, M = x.shape[0], x.shape[1], x.shape[2]
        Sk = c_kv.shape[1]
        H, D = w_q.shape[1], w_q.shape[2]
        q = torch.einsum("bqm,mhd->bhqd", x, w_q)
        k = torch.einsum("bkc,chd->bhkd", c_kv, w_uk)
        v = torch.einsum("bkc,chd->bhkd", c_kv, w_uv)
        scores = torch.einsum("bhqd,bhkd->bhqk", q, k) * (D ** -0.5)
        off = Sk - Sq
        qi = torch.arange(Sq)[:, None]
        kj = torch.arange(Sk)[None, :]
        cmask = kj <= (qi + off)
        scores = torch.where(cmask[None, None], scores, -1e9)
        p = _softmax(scores, axis=-1)
        ctx = torch.einsum("bhqk,bhkd->bhqd", p, v)
        return torch.einsum("bhqd,hdm->bqm", ctx, w_o)

    def _B_ref(x, w1, b1, w2, b2):
        h = torch.einsum("bsd,df->bsf", x, w1) + b1
        zero = torch.zeros_like(h)
        r = torch.where(h > zero, h, zero)
        h = r * r
        out = torch.einsum("bsf,fd->bsd", h, w2) + b2
        return out

    x = A_x
    g = _sigmoid((x * gate_w).sum(-1, keepdims=True))
    a = _A_ref(x, A_c_kv, A_w_q, A_w_uk, A_w_uv, A_w_o)
    b = _B_ref(x, B_w1, B_b1, B_w2, B_b2)
    return g * a + (1 - g) * b
```

```cpp
#include <hip/hip_runtime.h>
#include <hip/hip_bf16.h>
#include <stdint.h>

typedef unsigned short u16;
__device__ __forceinline__ float bf2f(u16 v) { return __uint_as_float(((unsigned)v) << 16); }

constexpr int B = 2, H = 32, SK = 8192, D = 128, M = 576, DC = 512, F = 4608;

__global__ void k_q(const u16* x, const u16* wq, float* q) {
  int col = blockIdx.x * 256 + threadIdx.x;
  float a0 = 0, a1 = 0;
  for (int m = 0; m < M; ++m) {
    float w = bf2f(wq[(size_t)m * 4096 + col]);
    a0 += bf2f(x[m]) * w; a1 += bf2f(x[M + m]) * w;
  }
  q[col] = a0; q[4096 + col] = a1;
}
__global__ void k_qabs(const float* q, const u16* wuk, float* qabs) {
  int idx = blockIdx.x * 256 + threadIdx.x;
  int c = idx >> 5, h = idx & 31;
  const u16* w = wuk + (size_t)c * 4096 + h * 128;
  float a0 = 0, a1 = 0;
  for (int d = 0; d < D; ++d) { float ww = bf2f(w[d]); a0 += q[h * 128 + d] * ww; a1 += q[4096 + h * 128 + d] * ww; }
  const float sc = 0.08838834764831845f;
  qabs[(0 * 32 + h) * 512 + c] = a0 * sc; qabs[(1 * 32 + h) * 512 + c] = a1 * sc;
}
__global__ void k_scores(const float* qabs, const u16* ckv, float* sc) {
  int k = blockIdx.x * 64 + (threadIdx.x & 63); int b = blockIdx.y; int hq = threadIdx.x >> 6;
  float acc[8] = {0};
  const u16* row = ckv + ((size_t)b * SK + k) * DC;
  for (int c = 0; c < DC; ++c) {
    float v = bf2f(row[c]);
    for (int i = 0; i < 8; ++i) acc[i] += v * qabs[(b * 32 + hq * 8 + i) * 512 + c];
  }
  for (int i = 0; i < 8; ++i) sc[((size_t)b * 32 + hq * 8 + i) * SK + k] = acc[i];
}
__global__ void k_softmax(float* sc) {
  __shared__ float red[256];
  float* s = sc + (size_t)blockIdx.x * SK;
  float m = -1e30f;
  for (int i = threadIdx.x; i < SK; i += 256) m = fmaxf(m, s[i]);
  red[threadIdx.x] = m; __syncthreads();
  for (int o = 128; o > 0; o >>= 1) { if (threadIdx.x < o) red[threadIdx.x] = fmaxf(red[threadIdx.x], red[threadIdx.x + o]); __syncthreads(); }
  m = red[0]; __syncthreads();
  float l = 0;
  for (int i = threadIdx.x; i < SK; i += 256) { float e = expf(s[i] - m); s[i] = e; l += e; }
  red[threadIdx.x] = l; __syncthreads();
  for (int o = 128; o > 0; o >>= 1) { if (threadIdx.x < o) red[threadIdx.x] += red[threadIdx.x + o]; __syncthreads(); }
  l = red[0];
  for (int i = threadIdx.x; i < SK; i += 256) s[i] /= l;
}
__global__ void k_pv(const float* p, const u16* ckv, float* part) {
  int bh = blockIdx.x, sp = blockIdx.y, b = bh >> 5, c = threadIdx.x;
  float a = 0;
  for (int k = sp * 512; k < sp * 512 + 512; ++k) a += p[(size_t)bh * SK + k] * bf2f(ckv[((size_t)b * SK + k) * DC + c]);
  part[((size_t)bh * 16 + sp) * 512 + c] = a;
}
__global__ void k_ctx(const float* part, const u16* wuv, float* ctx) {
  __shared__ float cl[512];
  int bh = blockIdx.x, h = bh & 31, d = threadIdx.x;
  for (int c = d; c < 512; c += 128) { float a = 0; for (int s = 0; s < 16; ++s) a += part[((size_t)bh * 16 + s) * 512 + c]; cl[c] = a; }
  __syncthreads();
  float a = 0;
  for (int c = 0; c < 512; ++c) a += cl[c] * bf2f(wuv[(size_t)c * 4096 + h * 128 + d]);
  ctx[bh * 128 + d] = a;
}
__global__ void k_h(const u16* x, const u16* w1, const u16* b1, float* hbuf) {
  int f = blockIdx.x * 256 + threadIdx.x;
  float a0 = 0, a1 = 0;
  for (int m = 0; m < M; ++m) { float w = bf2f(w1[(size_t)m * F + f]); a0 += bf2f(x[m]) * w; a1 += bf2f(x[M + m]) * w; }
  a0 += bf2f(b1[f]); a1 += bf2f(b1[f]);
  a0 = fmaxf(a0, 0.f); a1 = fmaxf(a1, 0.f);
  hbuf[f] = a0 * a0; hbuf[F + f] = a1 * a1;
}
__global__ void k_out(const u16* x, const u16* gw, const float* ctx, const u16* wo, const float* hbuf, const u16* w2, const u16* b2, __hip_bfloat16* out) {
  int n = blockIdx.x * 64 + threadIdx.x; int b = blockIdx.y;
  float g = 0; for (int m = 0; m < M; ++m) g += bf2f(x[b * M + m]) * bf2f(gw[m]);
  g = 1.f / (1.f + expf(-g));
  float a = 0; for (int i = 0; i < 4096; ++i) a += ctx[b * 4096 + i] * bf2f(wo[(size_t)i * M + n]);
  float o = 0; for (int i = 0; i < F; ++i) o += hbuf[b * F + i] * bf2f(w2[(size_t)i * M + n]);
  o += bf2f(b2[n]);
  out[b * M + n] = __float2bfloat16(g * a + (1.f - g) * o);
}

extern "C" void kernel_launch(void* const* d_in, const int* in_sizes, int n_in, void* d_out, int out_size, void* d_ws, size_t ws_size, hipStream_t stream) {
  const u16* x = (const u16*)d_in[0]; const u16* ckv = (const u16*)d_in[1]; const u16* wq = (const u16*)d_in[2];
  const u16* wuk = (const u16*)d_in[3]; const u16* wuv = (const u16*)d_in[4]; const u16* wo = (const u16*)d_in[5];
  const u16* w1 = (const u16*)d_in[7]; const u16* b1 = (const u16*)d_in[8]; const u16* w2 = (const u16*)d_in[9];
  const u16* b2 = (const u16*)d_in[10]; const u16* gw = (const u16*)d_in[11];
  float* ws = (float*)d_ws;
  float* q = ws; float* qabs = q + 8192; float* sc = qabs + 32768; float* part = sc + (size_t)64 * SK; float* ctx = part + 64 * 16 * 512; float* hbuf = ctx + 8192;
  k_q<<<16, 256, 0, stream>>>(x, wq, q);
  k_qabs<<<64, 256, 0, stream>>>(q, wuk, qabs);
  k_scores<<<dim3(128, 2), 256, 0, stream>>>(qabs, ckv, sc);
  k_softmax<<<64, 256, 0, stream>>>(sc);
  k_pv<<<dim3(64, 16), 512, 0, stream>>>(sc, ckv, part);
  k_ctx<<<64, 128, 0, stream>>>(part, wuv, ctx);
  k_h<<<18, 256, 0, stream>>>(x, w1, b1, hbuf);
  k_out<<<dim3(9, 2), 64, 0, stream>>>(x, gw, ctx, wo, hbuf, w2, b2, (__hip_bfloat16*)d_out);
}
```

```cpp
#include <hip/hip_runtime.h>
#include <hip/hip_bf16.h>
#include <stdint.h>

#define GAS __attribute__((address_space(1)))
#define LAS __attribute__((address_space(3)))
#define DI __device__ __forceinline__
typedef unsigned short u16;
typedef unsigned long long u64;
typedef unsigned v4u __attribute__((ext_vector_type(4)));
typedef unsigned v2u __attribute__((ext_vector_type(2)));
typedef float f32x2 __attribute__((ext_vector_type(2)));
typedef float f32x4 __attribute__((ext_vector_type(4)));
typedef float f32x16 __attribute__((ext_vector_type(16)));
typedef short bf16x8 __attribute__((ext_vector_type(8)));
typedef short s16x4 __attribute__((ext_vector_type(4)));
typedef __bf16 bf2_t __attribute__((ext_vector_type(2)));

constexpr int NM = 576, NH = 32, ND = 128, NC = 512, NSK = 8192, NF = 4608;
constexpr int WS_EPOCH = 0;
constexpr int WS_QABS = 4096;
constexpr int WS_STAT = WS_QABS + 131072;
constexpr int WS_POUT = WS_STAT + 65536;
constexpr int WS_OPART = WS_POUT + 72 * 128 * 128;
constexpr int WS_STAMP = WS_OPART + 4194304;
constexpr int WS_BYTES = WS_STAMP + 256 * 256;
constexpr int AUX_SC1 = 16;
constexpr unsigned SPIN_CAP = 1u << 17;

#define T_GATHER 150
#define T_HEADB 300
#define T_MLP 380

#define STAMP(i) do {} while (0)
#define STAMPW(wv, i) do {} while (0)

DI unsigned fsw(unsigned r) { return ((r & 3u) << 2) | ((((r >> 3) ^ (r >> 2)) & 1u) << 1) | ((r >> 3) & 1u); }
DI float bflo(unsigned v) { return __uint_as_float(v << 16); }
DI float bfhi(unsigned v) { return __uint_as_float(v & 0xffff0000u); }
DI unsigned pk2(float a, float b) { f32x2 f = {a, b}; bf2_t r = __builtin_convertvector(f, bf2_t); return __builtin_bit_cast(unsigned, r); }
DI unsigned bf1(float a) { return pk2(a, 0.f) & 0xffffu; }
DI f32x4 mfma16(v4u a, v4u b, f32x4 c) { return __builtin_amdgcn_mfma_f32_16x16x32_bf16(__builtin_bit_cast(bf16x8, a), __builtin_bit_cast(bf16x8, b), c, 0, 0, 0); }
DI f32x16 mfma32(v4u a, v4u b, f32x16 c) { return __builtin_amdgcn_mfma_f32_32x32x16_bf16(__builtin_bit_cast(bf16x8, a), __builtin_bit_cast(bf16x8, b), c, 0, 0, 0); }
DI v2u trread(LAS char* p) { s16x4 r = __builtin_amdgcn_ds_read_tr16_b64_v4i16((LAS s16x4*)p); return __builtin_bit_cast(v2u, r); }
DI v4u trfrag(LAS char* p0, LAS char* p1) { v2u a = trread(p0), b = trread(p1); return v4u{a.x, a.y, b.x, b.y}; }
DI v4u lds128(LAS char* p) { return *(LAS v4u*)p; }
DI void dma16(const u16* g, LAS char* l) { __builtin_amdgcn_global_load_lds((const GAS unsigned*)g, (LAS unsigned*)l, 16, 0, 0); }
DI v4u gld(const u16* p) { return *(const GAS v4u*)p; }
template <int CTRL> DI float dppf(float x) { return __builtin_bit_cast(float, __builtin_amdgcn_mov_dpp(__builtin_bit_cast(int, x), CTRL, 0xf, 0xf, true)); }
template <int CTRL> DI unsigned dppu(unsigned x) { return (unsigned)__builtin_amdgcn_mov_dpp((int)x, CTRL, 0xf, 0xf, true); }
DI float sum32(float x) { auto t = __builtin_amdgcn_permlane32_swap(__float_as_uint(x), __float_as_uint(x), false, false); return __uint_as_float(t[0]) + __uint_as_float(t[1]); }
DI float max32(float x) { auto t = __builtin_amdgcn_permlane32_swap(__float_as_uint(x), __float_as_uint(x), false, false); return fmaxf(__uint_as_float(t[0]), __uint_as_float(t[1])); }
DI float sum16(float x) { auto t = __builtin_amdgcn_permlane16_swap(__float_as_uint(x), __float_as_uint(x), false, false); return __uint_as_float(t[0]) + __uint_as_float(t[1]); }
DI float max16(float x) { auto t = __builtin_amdgcn_permlane16_swap(__float_as_uint(x), __float_as_uint(x), false, false); return fmaxf(__uint_as_float(t[0]), __uint_as_float(t[1])); }
DI float wave_sum(float x) { x += dppf<0xB1>(x); x += dppf<0x4E>(x); x += dppf<0x141>(x); x += dppf<0x128>(x); x = sum16(x); return sum32(x); }
DI float wave_max(float x) { x = fmaxf(x, dppf<0xB1>(x)); x = fmaxf(x, dppf<0x4E>(x)); x = fmaxf(x, dppf<0x141>(x)); x = fmaxf(x, dppf<0x128>(x)); x = max16(x); return max32(x); }
DI void wait_until(u64 t) { while (__builtin_amdgcn_s_memrealtime() < t) __builtin_amdgcn_s_sleep(1); }
#define DRAIN() asm volatile("s_waitcnt vmcnt(0)" ::: "memory")
#define CBAR() asm volatile("" ::: "memory")
#define SCHED() __builtin_amdgcn_sched_barrier(0)
#define LBAR() asm volatile("s_waitcnt lgkmcnt(0)\n\ts_barrier" ::: "memory")

DI int tile_lane_off(int lane, int ld) { return (8 * ((lane >> 3) & 1) + ((lane >> 1) & 3)) * ld + 16 * (lane >> 4) + 8 * (lane & 1); }

__global__ void __launch_bounds__(512, 2)
fused_kernel(const u16* __restrict__ x, const u16* __restrict__ ckv, const u16* __restrict__ wq, const u16* __restrict__ wuk,
             const u16* __restrict__ wuv, const u16* __restrict__ wo, const u16* __restrict__ w1, const u16* __restrict__ b1,
             const u16* __restrict__ w2, const u16* __restrict__ b2, const u16* __restrict__ gw, u16* __restrict__ out, char* ws) {
  __shared__ __attribute__((aligned(16))) char smem[163840];
  LAS char* L = (LAS char*)smem;
  const int tid = threadIdx.x, lane = tid & 63, w = __builtin_amdgcn_readfirstlane(tid >> 6);
  const int bid = blockIdx.x, xcd = bid & 7, kk = bid >> 3;
  const u64 t0 = __builtin_amdgcn_s_memrealtime();
  const __amdgpu_buffer_rsrc_t rs = __builtin_amdgcn_make_buffer_rsrc((void*)ws, 0, WS_BYTES, 0x00020000);
#define LOAD_EPOCH() (__hip_atomic_load((GAS u64*)(ws + WS_EPOCH), __ATOMIC_RELAXED, __HIP_MEMORY_SCOPE_AGENT) + 1ull)
  STAMP(0);
  const int g4 = lane >> 4, col = lane & 15;

  if (kk < 8) {
    const int h = xcd * 4 + (kk >> 1), j = kk & 1;
    constexpr int MISC = 147456, RED = MISC, CTXA = MISC + 8192, CTX2 = MISC + 10240, QA = MISC + 11264;
    {
      const u16* src = wq + h * ND + tile_lane_off(lane, 4096) + (16 * (w & 1) + 4 * (w >> 2)) * 4096 + 64 * ((w >> 1) & 1);
#pragma unroll
      for (int i = 0; i < 18; ++i) dma16(src + (size_t)i * 32 * 4096, L + (i * 8 + w) * 1024);
    }
    v4u uk[2][4];
#pragma unroll
    for (int T = 0; T < 2; ++T)
#pragma unroll
      for (int ks = 0; ks < 4; ++ks) uk[T][ks] = gld(wuk + (size_t)(256 * j + 32 * w + 16 * T + col) * 4096 + h * ND + 32 * ks + 8 * g4);
    v4u xa[18];
#pragma unroll
    for (int i = 0; i < 18; ++i) xa[i] = gld(x + (col & 1) * NM + 32 * i + 8 * g4);
    const u64 epoch = LOAD_EPOCH();
    SCHED();
    DRAIN();
    __syncthreads();
    const unsigned tagA = (unsigned)epoch, tagB = (unsigned)(epoch >> 32);
    STAMP(1);
    {
      f32x4 acc = {0.f, 0.f, 0.f, 0.f};
      LAS char* p = L + (w >> 2) * 2048 + (lane >> 5) * 1024 + (w & 3) * 256 + (lane & 31) * 8;
#pragma unroll
      for (int i = 0; i < 18; ++i) acc = mfma16(xa[i], trfrag(p + i * 8192, p + i * 8192 + 4096), acc);
      const float v = (g4 & 1) ? acc[1] : acc[0];
      const unsigned hi = bf1(v);
      const unsigned o = (g4 & 2) ? bf1(v - bflo(hi)) : hi;
      *(LAS u16*)(L + QA + g4 * 256 + (16 * w + col) * 2) = (u16)o;
    }
    __syncthreads();
    {
      f32x4 a0 = {0.f, 0.f, 0.f, 0.f}, a1 = {0.f, 0.f, 0.f, 0.f};
#pragma unroll
      for (int ks = 0; ks < 4; ++ks) {
        const v4u qa = lds128(L + QA + (col & 3) * 256 + ks * 64 + g4 * 16);
        a0 = mfma16(qa, uk[0][ks], a0);
        a1 = mfma16(qa, uk[1][ks], a1);
      }
      const f32x4 a = (g4 & 2) ? a1 : a0;
      const float v = ((g4 & 1) ? (a[1] + a[3]) : (a[0] + a[2])) * (0.08838834764831845f * 1.4426950408889634f);
      const unsigned bits = bf1(v);
      const unsigned n1 = dppu<0x55>(bits), n2 = dppu<0xAA>(bits), n3 = dppu<0xFF>(bits);
      if ((lane & 3) == 0) {
        const v4u gr = {tagA, bits | (n1 << 16), tagB, n2 | (n3 << 16)};
        const int off = WS_QABS + (((g4 & 1) * 32 + h) * 128 + (256 * j + 32 * w + 16 * (g4 >> 1) + col) / 4) * 16;
        __builtin_amdgcn_raw_buffer_store_b128(gr, rs, off, 0, AUX_SC1);
      }
    }
    STAMP(2);
    wait_until(t0 + T_HEADB);
    __syncthreads();
    {
      const u16* src = wuv + (size_t)(256 * j) * 4096 + h * ND + tile_lane_off(lane, 4096) + (16 * (w & 1) + 4 * (w >> 2)) * 4096 + 64 * ((w >> 1) & 1);
#pragma unroll
      for (int i = 0; i < 8; ++i) dma16(src + (size_t)i * 32 * 4096, L + (i * 8 + w) * 1024);
      const u16* so = wo + (size_t)(h * ND) * NM + tile_lane_off(lane, NM) + (32 * (w >> 1) + 4 * (w & 1)) * NM;
#pragma unroll
      for (int c4 = 4; c4 < 9; ++c4)
#pragma unroll
        for (int gh = 0; gh < 2; ++gh) dma16(so + 16 * gh * NM + 64 * c4, L + ((c4 * 8 + w) * 2 + gh) * 1024);
    }
    asm volatile("s_waitcnt vmcnt(10)\n\ts_barrier" ::: "memory");
    v4u uv[8];
    {
      LAS char* p = L + (w >> 2) * 2048 + (lane >> 5) * 1024 + (w & 3) * 256 + (lane & 31) * 8;
#pragma unroll
      for (int i = 0; i < 8; ++i) {
        v2u a, b;
        asm volatile("ds_read_b64_tr_b16 %0, %2 offset:%3\n\tds_read_b64_tr_b16 %1, %2 offset:%4" : "=&v"(a), "=&v"(b) : "v"((unsigned)(size_t)p), "i"(i * 8192), "i"(i * 8192 + 4096) : "memory");
        uv[i] = v4u{a.x, a.y, b.x, b.y};
      }
      asm volatile("s_waitcnt lgkmcnt(0)" : "+v"(uv[0]), "+v"(uv[1]), "+v"(uv[2]), "+v"(uv[3]), "+v"(uv[4]), "+v"(uv[5]), "+v"(uv[6]), "+v"(uv[7]) :: "memory");
    }
    LBAR();
    {
      const u16* so = wo + (size_t)(h * ND) * NM + tile_lane_off(lane, NM) + (32 * (w >> 1) + 4 * (w & 1)) * NM;
#pragma unroll
      for (int c4 = 0; c4 < 4; ++c4)
#pragma unroll
        for (int gh = 0; gh < 2; ++gh) dma16(so + 16 * gh * NM + 64 * c4, L + ((c4 * 8 + w) * 2 + gh) * 1024);
    }
    DRAIN();
    STAMP(3);
    const int b = w >> 2, q4 = w & 3;
    float wgt;
    {
      const int off = WS_STAT + ((h * 2 + b) * 64 + lane) * 16;
      v4u g;
      for (unsigned spins = 0;; ++spins) {
        CBAR();
        g = __builtin_amdgcn_raw_buffer_load_b128(rs, off, 0, AUX_SC1);
        if (__all(g.x == tagA && g.z == tagB) || spins > SPIN_CAP) break;
        __builtin_amdgcn_s_sleep(2);
      }
      __builtin_amdgcn_fence(__ATOMIC_ACQUIRE, "wavefront");
      const float ms = __uint_as_float(g.y), ls = __uint_as_float(g.w);
      const float Mx = wave_max(ms);
      const float e = __builtin_amdgcn_exp2f(ms - Mx);
      const float Ls = wave_sum(e * ls);
      wgt = e / Ls;
    }
    STAMPW(0, 4);
    {
      v4u o[8];
      const int off = WS_OPART + ((((h * 2 + j) * 2 + b) * 64 + 16 * q4) * 512) + lane * 16;
#pragma unroll
      for (int i = 0; i < 8; ++i) o[i] = __builtin_amdgcn_raw_buffer_load_b128(rs, off + i * 1024, 0, AUX_SC1);
      float acc[8] = {0.f, 0.f, 0.f, 0.f, 0.f, 0.f, 0.f, 0.f};
#pragma unroll
      for (int i = 0; i < 8; ++i) {
        const float wa = __uint_as_float(__builtin_amdgcn_readlane(__float_as_uint(wgt), 16 * q4 + 2 * i));
        const float wb = __uint_as_float(__builtin_amdgcn_readlane(__float_as_uint(wgt), 16 * q4 + 2 * i + 1));
        const float ww = lane < 32 ? wa : wb;
        acc[0] += ww * bflo(o[i].x); acc[1] += ww * bfhi(o[i].x);
        acc[2] += ww * bflo(o[i].y); acc[3] += ww * bfhi(o[i].y);
        acc[4] += ww * bflo(o[i].z); acc[5] += ww * bfhi(o[i].z);
        acc[6] += ww * bflo(o[i].w); acc[7] += ww * bfhi(o[i].w);
      }
#pragma unroll
      for (int e = 0; e < 8; ++e) acc[e] = sum32(acc[e]);
      const f32x4 v = lane < 32 ? f32x4{acc[0], acc[1], acc[2], acc[3]} : f32x4{acc[4], acc[5], acc[6], acc[7]};
      *(LAS f32x4*)(L + RED + w * 1024 + (lane & 31) * 32 + (lane >> 5) * 16) = v;
    }
    __syncthreads();
    {
      const int bb = tid >> 8, c = tid & 255;
      LAS float* r = (LAS float*)(L + RED + bb * 4096) + c;
      const float v = (r[0] + r[256]) + (r[512] + r[768]);
      const unsigned hi = bf1(v);
      *(LAS u16*)(L + CTXA + bb * 512 + c * 2) = (u16)hi;
      *(LAS u16*)(L + CTXA + (2 + bb) * 512 + c * 2) = (u16)bf1(v - bflo(hi));
    }
    __syncthreads();
    {
      f32x4 acc = {0.f, 0.f, 0.f, 0.f};
#pragma unroll
      for (int i = 0; i < 8; ++i) acc = mfma16(lds128(L + CTXA + (col & 3) * 512 + i * 64 + g4 * 16), uv[i], acc);
      const float v = (g4 & 1) ? (acc[1] + acc[3]) : (acc[0] + acc[2]);
      const unsigned hi = bf1(v);
      const unsigned o = (g4 & 2) ? bf1(v - bflo(hi)) : hi;
      *(LAS u16*)(L + CTX2 + g4 * 256 + (16 * w + col) * 2) = (u16)o;
    }
    __syncthreads();
    {
      v4u ca[4];
#pragma unroll
      for (int ks = 0; ks < 4; ++ks) ca[ks] = lds128(L + CTX2 + (col & 3) * 256 + ks * 64 + g4 * 16);
      f32x4 acc[5];
#pragma unroll
      for (int i = 0; i < 5; ++i) {
        acc[i] = f32x4{0.f, 0.f, 0.f, 0.f};
        if (i < 4 || w < 4) {
          const int ct = w + 8 * i;
          LAS char* p = L + (ct >> 2) * 16384 + (lane >> 5) * 1024 + (ct & 3) * 256 + (lane & 31) * 8;
#pragma unroll
          for (int ks = 0; ks < 4; ++ks) acc[i] = mfma16(ca[ks], trfrag(p + ks * 4096, p + ks * 4096 + 2048), acc[i]);
        }
      }
      const f32x4 a = g4 == 0 ? acc[0] : g4 == 1 ? acc[1] : g4 == 2 ? acc[2] : acc[3];
      const int p = h * 2 + j;
      {
        const int ct = w + 8 * g4;
        const v4u gr = {tagA, __float_as_uint(a[0] + a[2]), tagB, __float_as_uint(a[1] + a[3])};
        __builtin_amdgcn_raw_buffer_store_b128(gr, rs, WS_POUT + ((2 * ct + (col >> 3)) * 128 + p) * 128 + (col & 7) * 16, 0, AUX_SC1);
      }
      if (w < 4 && g4 == 0) {
        const int ct = w + 32;
        const v4u gr = {tagA, __float_as_uint(acc[4][0] + acc[4][2]), tagB, __float_as_uint(acc[4][1] + acc[4][3])};
        __builtin_amdgcn_raw_buffer_store_b128(gr, rs, WS_POUT + ((2 * ct + (col >> 3)) * 128 + p) * 128 + (col & 7) * 16, 0, AUX_SC1);
      }
    }
    STAMP(5);
    __builtin_amdgcn_endpgm();
  } else if (kk < 24) {
    const int a = xcd * 16 + (kk - 8), b = a >> 6, s = a & 63;
    constexpr int QO = 131072, XCH = QO, PB = QO + 16384, MX = QO + 24576, LS = QO + 25600;
    if (w < 4) {
      const u16* src = ckv + ((size_t)b * NSK + 128 * s + 32 * w) * NC;
#pragma unroll
      for (int i = 0; i < 32; ++i) dma16(src + i * NC + ((lane ^ fsw(i & 15)) * 8), L + (32 * w + i) * 1024);
    }
    SCHED();
    const u64 epoch = LOAD_EPOCH();
    const unsigned tagA = (unsigned)epoch, tagB = (unsigned)(epoch >> 32);
    if (w < 4) {
      DRAIN();
      STAMPW(0, 1);
    } else {
      wait_until(t0 + T_GATHER);
      const int gl = ((w - 4) & 1) * 64 + lane, h0 = (w - 4) >> 1;
      const int off = WS_QABS + ((b * 32 + h0) * 128 + gl) * 16;
      v4u g[16];
      for (unsigned spins = 0;; ++spins) {
        CBAR();
        bool ok = true;
#pragma unroll
        for (int u = 0; u < 16; ++u) g[u] = __builtin_amdgcn_raw_buffer_load_b128(rs, off + u * 4096, 0, AUX_SC1);
#pragma unroll
        for (int u = 0; u < 16; ++u) ok &= (g[u].x == tagA) & (g[u].z == tagB);
        if (__all(ok) || spins > SPIN_CAP) break;
        __builtin_amdgcn_s_sleep(4);
      }
#pragma unroll
      for (int u = 0; u < 16; ++u) {
        const int hd = 2 * u + h0;
        *(LAS v2u*)(L + QO + hd * 1024 + (((gl >> 1) ^ fsw(hd & 15)) << 4) + (gl & 1) * 8) = v2u{g[u].y, g[u].w};
      }
      STAMPW(4, 2);
    }
    __syncthreads();
    STAMP(3);
    const int r32 = lane & 31, hf = lane >> 5, kt = w & 3, ch = w >> 2;
    const unsigned fr = fsw(r32 & 15);
    f32x16 S;
#pragma unroll
    for (int i = 0; i < 16; ++i) S[i] = 0.f;
    {
      const unsigned ob = r32 * 1024 + ch * 512 + ((hf ^ fr) << 4);
#pragma unroll
      for (int st = 0; st < 16; ++st) {
        const unsigned o = (ob ^ ((st & 7) << 5)) + (st >> 3) * 256;
        S = mfma32(lds128(L + kt * 32768 + o), lds128(L + QO + o), S);
      }
    }
    __syncthreads();
    {
      LAS f32x4* xw = (LAS f32x4*)(L + XCH + w * 2048 + lane * 16);
      if (ch == 0) { xw[0] = f32x4{S[8], S[9], S[10], S[11]}; xw[64] = f32x4{S[12], S[13], S[14], S[15]}; }
      else         { xw[0] = f32x4{S[0], S[1], S[2], S[3]};   xw[64] = f32x4{S[4], S[5], S[6], S[7]}; }
    }
    __syncthreads();
    float sv[8];
    {
      LAS f32x4* xr = (LAS f32x4*)(L + XCH + (w ^ 4) * 2048 + lane * 16);
      const f32x4 p0 = xr[0], p1 = xr[64];
#pragma unroll
      for (int e = 0; e < 4; ++e) {
        sv[e] = (ch == 0 ? S[e] : S[8 + e]) + p0[e];
        sv[4 + e] = (ch == 0 ? S[4 + e] : S[12 + e]) + p1[e];
      }
      float m = fmaxf(fmaxf(fmaxf(sv[0], sv[1]), fmaxf(sv[2], sv[3])), fmaxf(fmaxf(sv[4], sv[5]), fmaxf(sv[6], sv[7])));
      m = max32(m);
      if (lane < 32) *(LAS float*)(L + MX + lane * 32 + w * 4) = m;
    }
    __syncthreads();
    float Mx;
    {
      const f32x4 m0 = *(LAS f32x4*)(L + MX + r32 * 32), m1 = *(LAS f32x4*)(L + MX + r32 * 32 + 16);
      Mx = fmaxf(fmaxf(fmaxf(m0[0], m0[1]), fmaxf(m0[2], m0[3])), fmaxf(fmaxf(m1[0], m1[1]), fmaxf(m1[2], m1[3])));
      float l = 0.f;
#pragma unroll
      for (int e = 0; e < 8; ++e) { sv[e] = __builtin_amdgcn_exp2f(sv[e] - Mx); l += sv[e]; }
      l = sum32(l);
      if (lane < 32) *(LAS float*)(L + LS + lane * 32 + w * 4) = l;
#pragma unroll
      for (int i = 0; i < 2; ++i)
        *(LAS v2u*)(L + PB + r32 * 256 + (((4 * kt + 2 * ch + i) ^ fr) << 4) + hf * 8) = v2u{pk2(sv[4 * i], sv[4 * i + 1]), pk2(sv[4 * i + 2], sv[4 * i + 3])};
    }
    __syncthreads();
    STAMP(4);
    float Ls;
    f32x16 O0, O1;
#pragma unroll
    for (int i = 0; i < 16; ++i) { O0[i] = 0.f; O1[i] = 0.f; }
    {
      const f32x4 l0 = *(LAS f32x4*)(L + LS + r32 * 32), l1 = *(LAS f32x4*)(L + LS + r32 * 32 + 16);
      Ls = ((l0[0] + l0[1]) + (l0[2] + l0[3])) + ((l1[0] + l1[1]) + (l1[2] + l1[3]));
      const unsigned pb = PB + r32 * 256 + ((hf ^ fr) << 4);
      const int blk = (lane >> 4) & 1, q = (lane >> 2) & 3, p = lane & 3;
      unsigned va[2][2];
#pragma unroll
      for (int t = 0; t < 2; ++t)
#pragma unroll
        for (int cc = 0; cc < 2; ++cc)
          va[t][cc] = (8 * hf + 4 * t + q) * 1024 + (((8 * w + 4 * cc + 2 * blk + (p >> 1)) ^ (4 * q + 2 * (hf ^ t) + hf)) << 4) + 8 * (p & 1);
#pragma unroll
      for (int ks = 0; ks < 8; ++ks) {
        const v4u pf = lds128(L + (pb ^ (ks << 5)));
        O0 = mfma32(trfrag(L + va[0][0] + ks * 16384, L + va[1][0] + ks * 16384), pf, O0);
        O1 = mfma32(trfrag(L + va[0][1] + ks * 16384, L + va[1][1] + ks * 16384), pf, O1);
      }
    }
    __syncthreads();
#pragma unroll
    for (int gq = 0; gq < 4; ++gq) {
      *(LAS v2u*)(L + QO + r32 * 1024 + (((8 * w + gq) ^ fr) << 4) + 8 * hf) = v2u{pk2(O0[4 * gq], O0[4 * gq + 1]), pk2(O0[4 * gq + 2], O0[4 * gq + 3])};
      *(LAS v2u*)(L + QO + r32 * 1024 + (((8 * w + 4 + gq) ^ fr) << 4) + 8 * hf) = v2u{pk2(O1[4 * gq], O1[4 * gq + 1]), pk2(O1[4 * gq + 2], O1[4 * gq + 3])};
    }
    __syncthreads();
#pragma unroll
    for (int i = 0; i < 4; ++i) {
      const int hd = 4 * w + i;
      const v4u v = lds128(L + QO + hd * 1024 + lane * 16);
      const int lc = lane ^ fsw(hd & 15);
      __builtin_amdgcn_raw_buffer_store_b128(v, rs, WS_OPART + ((((hd * 2 + (lc >> 5)) * 2 + b) * 64 + s) * 512) + (lc & 31) * 16, 0, AUX_SC1);
    }
    STAMP(5);
    DRAIN();
    if ((lane >> 2) == w) {
      const v4u gr = {tagA, __float_as_uint(Mx), tagB, __float_as_uint(Ls)};
      __builtin_amdgcn_raw_buffer_store_b128(gr, rs, WS_STAT + ((lane * 2 + b) * 64 + s) * 16, 0, AUX_SC1);
    }
    STAMP(6);
    if ((a & 15) < 9) {
      const int r = (a >> 4) * 9 + (a & 15);
      float g0, g1;
      {
        float d0 = 0.f, d1 = 0.f;
#pragma unroll
        for (int i = 0; i < 2; ++i) {
          const int c = lane + 64 * i;
          if (c < 72) {
            const v4u xa = gld(x + c * 8), xb = gld(x + NM + c * 8), gg = gld(gw + c * 8);
#pragma unroll
            for (int e = 0; e < 4; ++e) {
              d0 += bflo(xa[e]) * bflo(gg[e]) + bfhi(xa[e]) * bfhi(gg[e]);
              d1 += bflo(xb[e]) * bflo(gg[e]) + bfhi(xb[e]) * bfhi(gg[e]);
            }
          }
        }
        d0 = wave_sum(d0); d1 = wave_sum(d1);
        g0 = 1.f / (1.f + __expf(-d0)); g1 = 1.f / (1.f + __expf(-d1));
      }
      const float bias = tid < 16 ? bflo((unsigned)b2[8 * r + (tid & 7)]) : 0.f;
      const int off = WS_POUT + r * 16384 + tid * 16;
      v4u ga, gb;
      for (unsigned spins = 0;; ++spins) {
        CBAR();
        ga = __builtin_amdgcn_raw_buffer_load_b128(rs, off, 0, AUX_SC1);
        gb = __builtin_amdgcn_raw_buffer_load_b128(rs, off + 8192, 0, AUX_SC1);
        if (__all(ga.x == tagA && ga.z == tagB && gb.x == tagA && gb.z == tagB) || spins > SPIN_CAP) break;
        __builtin_amdgcn_s_sleep(2);
      }
      STAMP(7);
      float v0 = g0 * __uint_as_float(ga.y) + (1.f - g0) * __uint_as_float(gb.y);
      float v1 = g1 * __uint_as_float(ga.w) + (1.f - g1) * __uint_as_float(gb.w);
      v0 += dppf<0x128>(v0); v1 += dppf<0x128>(v1);
      v0 = sum32(sum16(v0)); v1 = sum32(sum16(v1));
      if (lane < 8) { *(LAS float*)(L + (lane * 8 + w) * 4) = v0; *(LAS float*)(L + 256 + (lane * 8 + w) * 4) = v1; }
      __syncthreads();
      if (tid < 16) {
        const f32x4 p0 = *(LAS f32x4*)(L + tid * 32), p1 = *(LAS f32x4*)(L + tid * 32 + 16);
        const float v = ((p0[0] + p0[1]) + (p0[2] + p0[3])) + ((p1[0] + p1[1]) + (p1[2] + p1[3])) + (1.f - (tid < 8 ? g0 : g1)) * bias;
        out[(tid >> 3) * NM + 8 * r + (tid & 7)] = (u16)bf1(v);
      }
      if (r == 0 && tid == 0) __hip_atomic_store((GAS u64*)(ws + WS_EPOCH), epoch, __ATOMIC_RELAXED, __HIP_MEMORY_SCOPE_AGENT);
      STAMP(8);
    }
    __builtin_amdgcn_endpgm();
  } else {
    const int mi = xcd * 8 + (kk - 24), f0 = 72 * mi;
    constexpr int XS = 0, R1 = 8192, HS = R1 + 48 * 144 * 4, R2 = HS + 1024;
    const u64 epoch = LOAD_EPOCH();
    const unsigned tagA = (unsigned)epoch, tagB = (unsigned)(epoch >> 32);
    for (int i = tid; i < NM; i += 512) *(LAS f32x2*)(L + XS + i * 8) = f32x2{bflo((unsigned)x[i]), bflo((unsigned)x[NM + i])};
    const float bias = tid < 144 ? bflo((unsigned)b1[f0 + (tid % 72)]) : 0.f;
    wait_until(t0 + T_MLP);
    const int tt = tid < 432 ? tid : 431;
    const int c1 = tt % 9, gr1 = tt / 9, c2 = tt % 72, gr2 = tt / 72;
    v4u wa[12], wb[12];
#pragma unroll
    for (int i = 0; i < 12; ++i) wa[i] = gld(w1 + (size_t)(12 * gr1 + i) * NF + f0 + 8 * c1);
#pragma unroll
    for (int i = 0; i < 12; ++i) wb[i] = gld(w2 + (size_t)(f0 + 12 * gr2 + i) * NM + 8 * c2);
    SCHED();
    LBAR();
    {
      f32x2 acc[8];
#pragma unroll
      for (int e = 0; e < 8; ++e) acc[e] = f32x2{0.f, 0.f};
#pragma unroll
      for (int i = 0; i < 12; ++i) {
        const f32x2 xv = *(LAS f32x2*)(L + XS + (12 * gr1 + i) * 8);
#pragma unroll
        for (int e = 0; e < 4; ++e) { acc[2 * e] += xv * bflo(wa[i][e]); acc[2 * e + 1] += xv * bfhi(wa[i][e]); }
      }
      {
        LAS f32x4* r = (LAS f32x4*)(L + R1 + gr1 * 576 + c1 * 32);
        r[0] = f32x4{acc[0].x, acc[1].x, acc[2].x, acc[3].x}; r[1] = f32x4{acc[4].x, acc[5].x, acc[6].x, acc[7].x};
        r[18] = f32x4{acc[0].y, acc[1].y, acc[2].y, acc[3].y}; r[19] = f32x4{acc[4].y, acc[5].y, acc[6].y, acc[7].y};
      }
    }
    LBAR();
    STAMP(1);
    if (tid < 144) {
      float v = bias;
      LAS float* r = (LAS float*)(L + R1) + tid;
#pragma unroll
      for (int i = 0; i < 48; ++i) v += r[i * 144];
      v = fmaxf(v, 0.f);
      *(LAS float*)(L + HS + (tid % 72) * 8 + (tid / 72) * 4) = v * v;
    }
    LBAR();
    {
      f32x2 acc[8];
#pragma unroll
      for (int e = 0; e < 8; ++e) acc[e] = f32x2{0.f, 0.f};
#pragma unroll
      for (int i = 0; i < 12; ++i) {
        const f32x2 hv = *(LAS f32x2*)(L + HS + (12 * gr2 + i) * 8);
#pragma unroll
        for (int e = 0; e < 4; ++e) { acc[2 * e] += hv * bflo(wb[i][e]); acc[2 * e + 1] += hv * bfhi(wb[i][e]); }
      }
      {
        LAS f32x4* r = (LAS f32x4*)(L + R2 + gr2 * 4608 + c2 * 64);
#pragma unroll
        for (int e = 0; e < 4; ++e) r[e] = f32x4{acc[2 * e].x, acc[2 * e].y, acc[2 * e + 1].x, acc[2 * e + 1].y};
      }
    }
    LBAR();
    for (int n = tid; n < NM; n += 512) {
      f32x2 v = {0.f, 0.f};
#pragma unroll
      for (int i = 0; i < 6; ++i) v += *(LAS f32x2*)(L + R2 + i * 4608 + n * 8);
      const v4u gr = {tagA, __float_as_uint(v.x), tagB, __float_as_uint(v.y)};
      __builtin_amdgcn_raw_buffer_store_b128(gr, rs, WS_POUT + ((n >> 3) * 128 + 64 + mi) * 128 + (n & 7) * 16, 0, AUX_SC1);
    }
    STAMP(2);
  }
}

extern "C" void kernel_launch(void* const* d_in, const int* in_sizes, int n_in, void* d_out, int out_size, void* d_ws, size_t ws_size,
                              hipStream_t stream) {
  fused_kernel<<<256, 512, 0, stream>>>((const u16*)d_in[0], (const u16*)d_in[1], (const u16*)d_in[2], (const u16*)d_in[3],
                                        (const u16*)d_in[4], (const u16*)d_in[5], (const u16*)d_in[7], (const u16*)d_in[8],
                                        (const u16*)d_in[9], (const u16*)d_in[10], (const u16*)d_in[11], (u16*)d_out, (char*)d_ws);
}
```

```cpp
#include <hip/hip_runtime.h>
#include <hip/hip_bf16.h>
#include <stdint.h>

#define GAS __attribute__((address_space(1)))
#define LAS __attribute__((address_space(3)))
#define DI __device__ __forceinline__
typedef unsigned short u16;
typedef unsigned long long u64;
typedef unsigned v4u __attribute__((ext_vector_type(4)));
typedef unsigned v2u __attribute__((ext_vector_type(2)));
typedef float f32x2 __attribute__((ext_vector_type(2)));
typedef float f32x4 __attribute__((ext_vector_type(4)));
typedef float f32x16 __attribute__((ext_vector_type(16)));
typedef short bf16x8 __attribute__((ext_vector_type(8)));
typedef short s16x4 __attribute__((ext_vector_type(4)));
typedef __bf16 bf2_t __attribute__((ext_vector_type(2)));

constexpr int NM = 576, NH = 32, ND = 128, NC = 512, NSK = 8192, NF = 4608;
constexpr int WS_EPOCH = 0;
constexpr int WS_QABS = 4096;
constexpr int WS_STAT = WS_QABS + 131072;
constexpr int WS_POUT = WS_STAT + 65536;
constexpr int WS_OPART = WS_POUT + 72 * 128 * 128;
constexpr int WS_STAMP = WS_OPART + 4194304;
constexpr int WS_BYTES = WS_STAMP + 256 * 256;
constexpr int AUX_SC1 = 16;
constexpr unsigned SPIN_CAP = 1u << 17;

#define T_GATHER 150
#define T_HEADB 300
#define T_MLP 380

#define STAMP(i) do {} while (0)
#define STAMPW(wv, i) do {} while (0)

DI unsigned fsw(unsigned r) { return ((r & 3u) << 2) | ((((r >> 3) ^ (r >> 2)) & 1u) << 1) | ((r >> 3) & 1u); }
DI float bflo(unsigned v) { return __uint_as_float(v << 16); }
DI float bfhi(unsigned v) { return __uint_as_float(v & 0xffff0000u); }
DI unsigned pk2(float a, float b) { f32x2 f = {a, b}; bf2_t r = __builtin_convertvector(f, bf2_t); return __builtin_bit_cast(unsigned, r); }
DI unsigned bf1(float a) { return pk2(a, 0.f) & 0xffffu; }
DI f32x4 mfma16(v4u a, v4u b, f32x4 c) { return __builtin_amdgcn_mfma_f32_16x16x32_bf16(__builtin_bit_cast(bf16x8, a), __builtin_bit_cast(bf16x8, b), c, 0, 0, 0); }
DI f32x16 mfma32(v4u a, v4u b, f32x16 c) { return __builtin_amdgcn_mfma_f32_32x32x16_bf16(__builtin_bit_cast(bf16x8, a), __builtin_bit_cast(bf16x8, b), c, 0, 0, 0); }
DI v2u trread(LAS char* p) { s16x4 r = __builtin_amdgcn_ds_read_tr16_b64_v4i16((LAS s16x4*)p); return __builtin_bit_cast(v2u, r); }
DI v4u trfrag(LAS char* p0, LAS char* p1) { v2u a = trread(p0), b = trread(p1); return v4u{a.x, a.y, b.x, b.y}; }
DI v4u lds128(LAS char* p) { return *(LAS v4u*)p; }
DI void dma16(const u16* g, LAS char* l) { __builtin_amdgcn_global_load_lds((const GAS unsigned*)g, (LAS unsigned*)l, 16, 0, 2); }
DI v4u gld(const u16* p) { return *(const GAS v4u*)p; }
DI v4u gldnt(const u16* p) { return __builtin_nontemporal_load((const GAS v4u*)p); }
template <int CTRL> DI float dppf(float x) { return __builtin_bit_cast(float, __builtin_amdgcn_mov_dpp(__builtin_bit_cast(int, x), CTRL, 0xf, 0xf, true)); }
template <int CTRL> DI unsigned dppu(unsigned x) { return (unsigned)__builtin_amdgcn_mov_dpp((int)x, CTRL, 0xf, 0xf, true); }
DI float sum32(float x) { auto t = __builtin_amdgcn_permlane32_swap(__float_as_uint(x), __float_as_uint(x), false, false); return __uint_as_float(t[0]) + __uint_as_float(t[1]); }
DI float max32(float x) { auto t = __builtin_amdgcn_permlane32_swap(__float_as_uint(x), __float_as_uint(x), false, false); return fmaxf(__uint_as_float(t[0]), __uint_as_float(t[1])); }
DI float sum16(float x) { auto t = __builtin_amdgcn_permlane16_swap(__float_as_uint(x), __float_as_uint(x), false, false); return __uint_as_float(t[0]) + __uint_as_float(t[1]); }
DI float max16(float x) { auto t = __builtin_amdgcn_permlane16_swap(__float_as_uint(x), __float_as_uint(x), false, false); return fmaxf(__uint_as_float(t[0]), __uint_as_float(t[1])); }
DI float wave_sum(float x) { x += dppf<0xB1>(x); x += dppf<0x4E>(x); x += dppf<0x141>(x); x += dppf<0x128>(x); x = sum16(x); return sum32(x); }
DI float wave_max(float x) { x = fmaxf(x, dppf<0xB1>(x)); x = fmaxf(x, dppf<0x4E>(x)); x = fmaxf(x, dppf<0x141>(x)); x = fmaxf(x, dppf<0x128>(x)); x = max16(x); return max32(x); }
DI void wait_until(u64 t) { while (__builtin_amdgcn_s_memrealtime() < t) __builtin_amdgcn_s_sleep(1); }
#define DRAIN() asm volatile("s_waitcnt vmcnt(0)" ::: "memory")
#define CBAR() asm volatile("" ::: "memory")
#define SCHED() __builtin_amdgcn_sched_barrier(0)
#define LBAR() asm volatile("s_waitcnt lgkmcnt(0)\n\ts_barrier" ::: "memory")

DI int tile_lane_off(int lane, int ld) { const int r8 = lane >> 3; return (8 * (r8 >> 2) + (r8 & 3)) * ld + 8 * ((lane & 7) ^ ((r8 >> 1) << 1)); }
DI int tile_tr_off(int lane, int ct) { const int r8 = 4 * ((lane >> 4) & 1) + ((lane >> 2) & 3); return (lane >> 5) * 1024 + r8 * 128 + ((ct ^ (r8 >> 1)) * 32) + (lane & 3) * 8; }

__global__ void __launch_bounds__(512, 2)
fused_kernel(const u16* __restrict__ x, const u16* __restrict__ ckv, const u16* __restrict__ wq, const u16* __restrict__ wuk,
             const u16* __restrict__ wuv, const u16* __restrict__ wo, const u16* __restrict__ w1, const u16* __restrict__ b1,
             const u16* __restrict__ w2, const u16* __restrict__ b2, const u16* __restrict__ gw, u16* __restrict__ out, char* ws) {
  __shared__ __attribute__((aligned(16))) char smem[163840];
  LAS char* L = (LAS char*)smem;
  const int tid = threadIdx.x, lane = tid & 63, w = __builtin_amdgcn_readfirstlane(tid >> 6);
  const int bid = blockIdx.x, xcd = bid & 7, kk = bid >> 3;
  const u64 t0 = __builtin_amdgcn_s_memrealtime();
  const __amdgpu_buffer_rsrc_t rs = __builtin_amdgcn_make_buffer_rsrc((void*)ws, 0, WS_BYTES, 0x00020000);
#define LOAD_EPOCH() (__hip_atomic_load((GAS u64*)(ws + WS_EPOCH), __ATOMIC_RELAXED, __HIP_MEMORY_SCOPE_AGENT) + 1ull)
  STAMP(0);
  const int g4 = lane >> 4, col = lane & 15;

  if (kk < 8) {
    const int h = xcd * 4 + (kk >> 1), j = kk & 1;
    constexpr int MISC = 147456, RED = MISC, CTXA = MISC + 8192, CTX2 = MISC + 10240, QA = MISC + 11264;
    {
      const u16* src = wq + h * ND + tile_lane_off(lane, 4096) + (16 * (w & 1) + 4 * (w >> 2)) * 4096 + 64 * ((w >> 1) & 1);
#pragma unroll
      for (int i = 0; i < 18; ++i) dma16(src + (size_t)i * 32 * 4096, L + (i * 8 + w) * 1024);
    }
    v4u uk[2][4];
#pragma unroll
    for (int T = 0; T < 2; ++T)
#pragma unroll
      for (int ks = 0; ks < 4; ++ks) uk[T][ks] = gldnt(wuk + (size_t)(256 * j + 32 * w + 16 * T + (lane >> 2)) * 4096 + h * ND + 32 * ks + 8 * (lane & 3));
    v4u xa[18];
#pragma unroll
    for (int i = 0; i < 18; ++i) xa[i] = gld(x + (col & 1) * NM + 32 * i + 8 * g4);
    const u64 epoch = LOAD_EPOCH();
    SCHED();
    DRAIN();
    {
      const int bi = (col * 4 + g4) * 4;
#pragma unroll
      for (int T = 0; T < 2; ++T)
#pragma unroll
        for (int ks = 0; ks < 4; ++ks)
#pragma unroll
          for (int e = 0; e < 4; ++e) uk[T][ks][e] = (unsigned)__builtin_amdgcn_ds_bpermute(bi, (int)uk[T][ks][e]);
    }
    __syncthreads();
    const unsigned tagA = (unsigned)epoch, tagB = (unsigned)(epoch >> 32);
    STAMP(1);
    {
      f32x4 acc = {0.f, 0.f, 0.f, 0.f};
      LAS char* p = L + (w >> 2) * 2048 + tile_tr_off(lane, w & 3);
#pragma unroll
      for (int i = 0; i < 18; ++i) acc = mfma16(xa[i], trfrag(p + i * 8192, p + i * 8192 + 4096), acc);
      const float v = (g4 & 1) ? acc[1] : acc[0];
      const unsigned hi = bf1(v);
      const unsigned o = (g4 & 2) ? bf1(v - bflo(hi)) : hi;
      *(LAS u16*)(L + QA + g4 * 256 + (16 * w + col) * 2) = (u16)o;
    }
    __syncthreads();
    {
      f32x4 a0 = {0.f, 0.f, 0.f, 0.f}, a1 = {0.f, 0.f, 0.f, 0.f};
#pragma unroll
      for (int ks = 0; ks < 4; ++ks) {
        const v4u qa = lds128(L + QA + (col & 3) * 256 + ks * 64 + g4 * 16);
        a0 = mfma16(qa, uk[0][ks], a0);
        a1 = mfma16(qa, uk[1][ks], a1);
      }
      const f32x4 a = (g4 & 2) ? a1 : a0;
      const float v = ((g4 & 1) ? (a[1] + a[3]) : (a[0] + a[2])) * (0.08838834764831845f * 1.4426950408889634f);
      const unsigned bits = bf1(v);
      const unsigned n1 = dppu<0x55>(bits), n2 = dppu<0xAA>(bits), n3 = dppu<0xFF>(bits);
      if ((lane & 3) == 0) {
        const v4u gr = {tagA, bits | (n1 << 16), tagB, n2 | (n3 << 16)};
        const int off = WS_QABS + (((g4 & 1) * 32 + h) * 128 + (256 * j + 32 * w + 16 * (g4 >> 1) + col) / 4) * 16;
        __builtin_amdgcn_raw_buffer_store_b128(gr, rs, off, 0, AUX_SC1);
      }
    }
    STAMP(2);
    wait_until(t0 + T_HEADB);
    __syncthreads();
    {
      const u16* src = wuv + (size_t)(256 * j) * 4096 + h * ND + tile_lane_off(lane, 4096) + (16 * (w & 1) + 4 * (w >> 2)) * 4096 + 64 * ((w >> 1) & 1);
#pragma unroll
      for (int i = 0; i < 8; ++i) dma16(src + (size_t)i * 32 * 4096, L + (i * 8 + w) * 1024);
      const u16* so = wo + (size_t)(h * ND) * NM + tile_lane_off(lane, NM) + (32 * (w >> 1) + 4 * (w & 1)) * NM;
#pragma unroll
      for (int c4 = 4; c4 < 9; ++c4)
#pragma unroll
        for (int gh = 0; gh < 2; ++gh) dma16(so + 16 * gh * NM + 64 * c4, L + ((c4 * 8 + w) * 2 + gh) * 1024);
    }
    asm volatile("s_waitcnt vmcnt(10)\n\ts_barrier" ::: "memory");
    v4u uv[8];
    {
      LAS char* p = L + (w >> 2) * 2048 + tile_tr_off(lane, w & 3);
#pragma unroll
      for (int i = 0; i < 8; ++i) {
        v2u a, b;
        asm volatile("ds_read_b64_tr_b16 %0, %2 offset:%3\n\tds_read_b64_tr_b16 %1, %2 offset:%4" : "=&v"(a), "=&v"(b) : "v"((unsigned)(size_t)p), "i"(i * 8192), "i"(i * 8192 + 4096) : "memory");
        uv[i] = v4u{a.x, a.y, b.x, b.y};
      }
      asm volatile("s_waitcnt lgkmcnt(0)" : "+v"(uv[0]), "+v"(uv[1]), "+v"(uv[2]), "+v"(uv[3]), "+v"(uv[4]), "+v"(uv[5]), "+v"(uv[6]), "+v"(uv[7]) :: "memory");
    }
    LBAR();
    {
      const u16* so = wo + (size_t)(h * ND) * NM + tile_lane_off(lane, NM) + (32 * (w >> 1) + 4 * (w & 1)) * NM;
#pragma unroll
      for (int c4 = 0; c4 < 4; ++c4)
#pragma unroll
        for (int gh = 0; gh < 2; ++gh) dma16(so + 16 * gh * NM + 64 * c4, L + ((c4 * 8 + w) * 2 + gh) * 1024);
    }
    DRAIN();
    STAMP(3);
    const int b = w >> 2, q4 = w & 3;
    float wgt;
    {
      const int off = WS_STAT + ((h * 2 + b) * 64 + lane) * 16;
      v4u g;
      for (unsigned spins = 0;; ++spins) {
        CBAR();
        g = __builtin_amdgcn_raw_buffer_load_b128(rs, off, 0, AUX_SC1);
        if (__all(g.x == tagA && g.z == tagB) || spins > SPIN_CAP) break;
        __builtin_amdgcn_s_sleep(2);
      }
      __builtin_amdgcn_fence(__ATOMIC_ACQUIRE, "wavefront");
      const float ms = __uint_as_float(g.y), ls = __uint_as_float(g.w);
      const float Mx = wave_max(ms);
      const float e = __builtin_amdgcn_exp2f(ms - Mx);
      const float Ls = wave_sum(e * ls);
      wgt = e / Ls;
    }
    STAMPW(0, 4);
    {
      v4u o[8];
      const int off = WS_OPART + ((((h * 2 + j) * 2 + b) * 64 + 16 * q4) * 512) + lane * 16;
#pragma unroll
      for (int i = 0; i < 8; ++i) o[i] = __builtin_amdgcn_raw_buffer_load_b128(rs, off + i * 1024, 0, AUX_SC1);
      float acc[8] = {0.f, 0.f, 0.f, 0.f, 0.f, 0.f, 0.f, 0.f};
#pragma unroll
      for (int i = 0; i < 8; ++i) {
        const float wa = __uint_as_float(__builtin_amdgcn_readlane(__float_as_uint(wgt), 16 * q4 + 2 * i));
        const float wb = __uint_as_float(__builtin_amdgcn_readlane(__float_as_uint(wgt), 16 * q4 + 2 * i + 1));
        const float ww = lane < 32 ? wa : wb;
        acc[0] += ww * bflo(o[i].x); acc[1] += ww * bfhi(o[i].x);
        acc[2] += ww * bflo(o[i].y); acc[3] += ww * bfhi(o[i].y);
        acc[4] += ww * bflo(o[i].z); acc[5] += ww * bfhi(o[i].z);
        acc[6] += ww * bflo(o[i].w); acc[7] += ww * bfhi(o[i].w);
      }
#pragma unroll
      for (int e = 0; e < 8; ++e) acc[e] = sum32(acc[e]);
      const f32x4 v = lane < 32 ? f32x4{acc[0], acc[1], acc[2], acc[3]} : f32x4{acc[4], acc[5], acc[6], acc[7]};
      *(LAS f32x4*)(L + RED + w * 1024 + (lane & 31) * 32 + (lane >> 5) * 16) = v;
    }
    __syncthreads();
    {
      const int bb = tid >> 8, c = tid & 255;
      LAS float* r = (LAS float*)(L + RED + bb * 4096) + c;
      const float v = (r[0] + r[256]) + (r[512] + r[768]);
      const unsigned hi = bf1(v);
      *(LAS u16*)(L + CTXA + bb * 512 + c * 2) = (u16)hi;
      *(LAS u16*)(L + CTXA + (2 + bb) * 512 + c * 2) = (u16)bf1(v - bflo(hi));
    }
    __syncthreads();
    {
      f32x4 acc = {0.f, 0.f, 0.f, 0.f};
#pragma unroll
      for (int i = 0; i < 8; ++i) acc = mfma16(lds128(L + CTXA + (col & 3) * 512 + i * 64 + g4 * 16), uv[i], acc);
      const float v = (g4 & 1) ? (acc[1] + acc[3]) : (acc[0] + acc[2]);
      const unsigned hi = bf1(v);
      const unsigned o = (g4 & 2) ? bf1(v - bflo(hi)) : hi;
      *(LAS u16*)(L + CTX2 + g4 * 256 + (16 * w + col) * 2) = (u16)o;
    }
    __syncthreads();
    {
      v4u ca[4];
#pragma unroll
      for (int ks = 0; ks < 4; ++ks) ca[ks] = lds128(L + CTX2 + (col & 3) * 256 + ks * 64 + g4 * 16);
      f32x4 acc[5];
#pragma unroll
      for (int i = 0; i < 5; ++i) {
        acc[i] = f32x4{0.f, 0.f, 0.f, 0.f};
        if (i < 4 || w < 4) {
          const int ct = w + 8 * i;
          LAS char* p = L + (ct >> 2) * 16384 + tile_tr_off(lane, w & 3);
#pragma unroll
          for (int ks = 0; ks < 4; ++ks) acc[i] = mfma16(ca[ks], trfrag(p + ks * 4096, p + ks * 4096 + 2048), acc[i]);
        }
      }
      const f32x4 a = g4 == 0 ? acc[0] : g4 == 1 ? acc[1] : g4 == 2 ? acc[2] : acc[3];
      const int p = h * 2 + j;
      {
        const int ct = w + 8 * g4;
        const v4u gr = {tagA, __float_as_uint(a[0] + a[2]), tagB, __float_as_uint(a[1] + a[3])};
        __builtin_amdgcn_raw_buffer_store_b128(gr, rs, WS_POUT + ((2 * ct + (col >> 3)) * 128 + p) * 128 + (col & 7) * 16, 0, AUX_SC1);
      }
      if (w < 4 && g4 == 0) {
        const int ct = w + 32;
        const v4u gr = {tagA, __float_as_uint(acc[4][0] + acc[4][2]), tagB, __float_as_uint(acc[4][1] + acc[4][3])};
        __builtin_amdgcn_raw_buffer_store_b128(gr, rs, WS_POUT + ((2 * ct + (col >> 3)) * 128 + p) * 128 + (col & 7) * 16, 0, AUX_SC1);
      }
    }
    STAMP(5);
    __builtin_amdgcn_endpgm();
  } else if (kk < 24) {
    const int a = xcd * 16 + (kk - 8), b = a >> 6, s = a & 63;
    constexpr int QO = 131072, XCH = QO, PB = QO + 16384, MX = QO + 24576, LS = QO + 25600;
    if (w < 4) {
      const u16* src = ckv + ((size_t)b * NSK + 128 * s + 32 * w) * NC;
#pragma unroll
      for (int i = 0; i < 32; ++i) dma16(src + i * NC + ((lane ^ fsw(i & 15)) * 8), L + (32 * w + i) * 1024);
    }
    SCHED();
    const u64 epoch = LOAD_EPOCH();
    const unsigned tagA = (unsigned)epoch, tagB = (unsigned)(epoch >> 32);
    if (w < 4) {
      DRAIN();
      STAMPW(0, 1);
    } else {
      wait_until(t0 + T_GATHER);
      const int gl = ((w - 4) & 1) * 64 + lane, h0 = (w - 4) >> 1;
      const int off = WS_QABS + ((b * 32 + h0) * 128 + gl) * 16;
      v4u g[16];
      for (unsigned spins = 0;; ++spins) {
        CBAR();
        bool ok = true;
#pragma unroll
        for (int u = 0; u < 16; ++u) g[u] = __builtin_amdgcn_raw_buffer_load_b128(rs, off + u * 4096, 0, AUX_SC1);
#pragma unroll
        for (int u = 0; u < 16; ++u) ok &= (g[u].x == tagA) & (g[u].z == tagB);
        if (__all(ok) || spins > SPIN_CAP) break;
        __builtin_amdgcn_s_sleep(4);
      }
#pragma unroll
      for (int u = 0; u < 16; ++u) {
        const int hd = 2 * u + h0;
        *(LAS v2u*)(L + QO + hd * 1024 + (((gl >> 1) ^ fsw(hd & 15)) << 4) + (gl & 1) * 8) = v2u{g[u].y, g[u].w};
      }
      STAMPW(4, 2);
    }
    __syncthreads();
    STAMP(3);
    const int r32 = lane & 31, hf = lane >> 5, kt = w & 3, ch = w >> 2;
    const unsigned fr = fsw(r32 & 15);
    f32x16 S;
#pragma unroll
    for (int i = 0; i < 16; ++i) S[i] = 0.f;
    {
      const unsigned ob = r32 * 1024 + ch * 512 + ((hf ^ fr) << 4);
#pragma unroll
      for (int st = 0; st < 16; ++st) {
        const unsigned o = (ob ^ ((st & 7) << 5)) + (st >> 3) * 256;
        S = mfma32(lds128(L + kt * 32768 + o), lds128(L + QO + o), S);
      }
    }
    __syncthreads();
    {
      LAS f32x4* xw = (LAS f32x4*)(L + XCH + w * 2048 + lane * 16);
      if (ch == 0) { xw[0] = f32x4{S[8], S[9], S[10], S[11]}; xw[64] = f32x4{S[12], S[13], S[14], S[15]}; }
      else         { xw[0] = f32x4{S[0], S[1], S[2], S[3]};   xw[64] = f32x4{S[4], S[5], S[6], S[7]}; }
    }
    __syncthreads();
    float sv[8];
    {
      LAS f32x4* xr = (LAS f32x4*)(L + XCH + (w ^ 4) * 2048 + lane * 16);
      const f32x4 p0 = xr[0], p1 = xr[64];
#pragma unroll
      for (int e = 0; e < 4; ++e) {
        sv[e] = (ch == 0 ? S[e] : S[8 + e]) + p0[e];
        sv[4 + e] = (ch == 0 ? S[4 + e] : S[12 + e]) + p1[e];
      }
      float m = fmaxf(fmaxf(fmaxf(sv[0], sv[1]), fmaxf(sv[2], sv[3])), fmaxf(fmaxf(sv[4], sv[5]), fmaxf(sv[6], sv[7])));
      m = max32(m);
      if (lane < 32) *(LAS float*)(L + MX + lane * 32 + w * 4) = m;
    }
    __syncthreads();
    float Mx;
    {
      const f32x4 m0 = *(LAS f32x4*)(L + MX + r32 * 32), m1 = *(LAS f32x4*)(L + MX + r32 * 32 + 16);
      Mx = fmaxf(fmaxf(fmaxf(m0[0], m0[1]), fmaxf(m0[2], m0[3])), fmaxf(fmaxf(m1[0], m1[1]), fmaxf(m1[2], m1[3])));
      float l = 0.f;
#pragma unroll
      for (int e = 0; e < 8; ++e) { sv[e] = __builtin_amdgcn_exp2f(sv[e] - Mx); l += sv[e]; }
      l = sum32(l);
      if (lane < 32) *(LAS float*)(L + LS + lane * 32 + w * 4) = l;
#pragma unroll
      for (int i = 0; i < 2; ++i)
        *(LAS v2u*)(L + PB + r32 * 256 + (((4 * kt + 2 * ch + i) ^ fr) << 4) + hf * 8) = v2u{pk2(sv[4 * i], sv[4 * i + 1]), pk2(sv[4 * i + 2], sv[4 * i + 3])};
    }
    __syncthreads();
    STAMP(4);
    float Ls;
    f32x16 O0, O1;
#pragma unroll
    for (int i = 0; i < 16; ++i) { O0[i] = 0.f; O1[i] = 0.f; }
    {
      const f32x4 l0 = *(LAS f32x4*)(L + LS + r32 * 32), l1 = *(LAS f32x4*)(L + LS + r32 * 32 + 16);
      Ls = ((l0[0] + l0[1]) + (l0[2] + l0[3])) + ((l1[0] + l1[1]) + (l1[2] + l1[3]));
      const unsigned pb = PB + r32 * 256 + ((hf ^ fr) << 4);
      const int blk = (lane >> 4) & 1, q = (lane >> 2) & 3, p = lane & 3;
      unsigned va[2][2];
#pragma unroll
      for (int t = 0; t < 2; ++t)
#pragma unroll
        for (int cc = 0; cc < 2; ++cc)
          va[t][cc] = (8 * hf + 4 * t + q) * 1024 + (((8 * w + 4 * cc + 2 * blk + (p >> 1)) ^ (4 * q + 2 * (hf ^ t) + hf)) << 4) + 8 * (p & 1);
#pragma unroll
      for (int ks = 0; ks < 8; ++ks) {
        const v4u pf = lds128(L + (pb ^ (ks << 5)));
        O0 = mfma32(trfrag(L + va[0][0] + ks * 16384, L + va[1][0] + ks * 16384), pf, O0);
        O1 = mfma32(trfrag(L + va[0][1] + ks * 16384, L + va[1][1] + ks * 16384), pf, O1);
      }
    }
    __syncthreads();
#pragma unroll
    for (int gq = 0; gq < 4; ++gq) {
      *(LAS v2u*)(L + QO + r32 * 1024 + (((8 * w + gq) ^ fr) << 4) + 8 * hf) = v2u{pk2(O0[4 * gq], O0[4 * gq + 1]), pk2(O0[4 * gq + 2], O0[4 * gq + 3])};
      *(LAS v2u*)(L + QO + r32 * 1024 + (((8 * w + 4 + gq) ^ fr) << 4) + 8 * hf) = v2u{pk2(O1[4 * gq], O1[4 * gq + 1]), pk2(O1[4 * gq + 2], O1[4 * gq + 3])};
    }
    __syncthreads();
#pragma unroll
    for (int i = 0; i < 4; ++i) {
      const int hd = 4 * w + i;
      const v4u v = lds128(L + QO + hd * 1024 + lane * 16);
      const int lc = lane ^ fsw(hd & 15);
      __builtin_amdgcn_raw_buffer_store_b128(v, rs, WS_OPART + ((((hd * 2 + (lc >> 5)) * 2 + b) * 64 + s) * 512) + (lc & 31) * 16, 0, AUX_SC1);
    }
    STAMP(5);
    DRAIN();
    if ((lane >> 2) == w) {
      const v4u gr = {tagA, __float_as_uint(Mx), tagB, __float_as_uint(Ls)};
      __builtin_amdgcn_raw_buffer_store_b128(gr, rs, WS_STAT + ((lane * 2 + b) * 64 + s) * 16, 0, AUX_SC1);
    }
    STAMP(6);
    if ((a & 15) < 9) {
      const int r = (a >> 4) * 9 + (a & 15);
      float g0, g1;
      {
        float d0 = 0.f, d1 = 0.f;
#pragma unroll
        for (int i = 0; i < 2; ++i) {
          const int c = lane + 64 * i;
          if (c < 72) {
            const v4u xa = gld(x + c * 8), xb = gld(x + NM + c * 8), gg = gld(gw + c * 8);
#pragma unroll
            for (int e = 0; e < 4; ++e) {
              d0 += bflo(xa[e]) * bflo(gg[e]) + bfhi(xa[e]) * bfhi(gg[e]);
              d1 += bflo(xb[e]) * bflo(gg[e]) + bfhi(xb[e]) * bfhi(gg[e]);
            }
          }
        }
        d0 = wave_sum(d0); d1 = wave_sum(d1);
        g0 = 1.f / (1.f + __expf(-d0)); g1 = 1.f / (1.f + __expf(-d1));
      }
      const float bias = tid < 16 ? bflo((unsigned)b2[8 * r + (tid & 7)]) : 0.f;
      const int off = WS_POUT + r * 16384 + tid * 16;
      v4u ga, gb;
      for (unsigned spins = 0;; ++spins) {
        CBAR();
        ga = __builtin_amdgcn_raw_buffer_load_b128(rs, off, 0, AUX_SC1);
        gb = __builtin_amdgcn_raw_buffer_load_b128(rs, off + 8192, 0, AUX_SC1);
        if (__all(ga.x == tagA && ga.z == tagB && gb.x == tagA && gb.z == tagB) || spins > SPIN_CAP) break;
        __builtin_amdgcn_s_sleep(2);
      }
      STAMP(7);
      float v0 = g0 * __uint_as_float(ga.y) + (1.f - g0) * __uint_as_float(gb.y);
      float v1 = g1 * __uint_as_float(ga.w) + (1.f - g1) * __uint_as_float(gb.w);
      v0 += dppf<0x128>(v0); v1 += dppf<0x128>(v1);
      v0 = sum32(sum16(v0)); v1 = sum32(sum16(v1));
      if (lane < 8) { *(LAS float*)(L + (lane * 8 + w) * 4) = v0; *(LAS float*)(L + 256 + (lane * 8 + w) * 4) = v1; }
      __syncthreads();
      if (tid < 16) {
        const f32x4 p0 = *(LAS f32x4*)(L + tid * 32), p1 = *(LAS f32x4*)(L + tid * 32 + 16);
        const float v = ((p0[0] + p0[1]) + (p0[2] + p0[3])) + ((p1[0] + p1[1]) + (p1[2] + p1[3])) + (1.f - (tid < 8 ? g0 : g1)) * bias;
        out[(tid >> 3) * NM + 8 * r + (tid & 7)] = (u16)bf1(v);
      }
      if (r == 0 && tid == 0) __hip_atomic_store((GAS u64*)(ws + WS_EPOCH), epoch, __ATOMIC_RELAXED, __HIP_MEMORY_SCOPE_AGENT);
      STAMP(8);
    }
    __builtin_amdgcn_endpgm();
  } else {
    const int mi = xcd * 8 + (kk - 24), f0 = 72 * mi;
    constexpr int XS = 0, R1 = 8192, HS = R1 + 48 * 144 * 4, R2 = HS + 1024;
    const u64 epoch = LOAD_EPOCH();
    const unsigned tagA = (unsigned)epoch, tagB = (unsigned)(epoch >> 32);
    for (int i = tid; i < NM; i += 512) *(LAS f32x2*)(L + XS + i * 8) = f32x2{bflo((unsigned)x[i]), bflo((unsigned)x[NM + i])};
    const float bias = tid < 144 ? bflo((unsigned)b1[f0 + (tid % 72)]) : 0.f;
    wait_until(t0 + T_MLP);
    const int tt = tid < 432 ? tid : 431;
    const int c1 = tt % 9, gr1 = tt / 9, c2 = tt % 72, gr2 = tt / 72;
    v4u wa[12], wb[12];
#pragma unroll
    for (int i = 0; i < 12; ++i) wa[i] = gldnt(w1 + (size_t)(12 * gr1 + i) * NF + f0 + 8 * c1);
#pragma unroll
    for (int i = 0; i < 12; ++i) wb[i] = gldnt(w2 + (size_t)(f0 + 12 * gr2 + i) * NM + 8 * c2);
    SCHED();
    LBAR();
    {
      f32x2 acc[8];
#pragma unroll
      for (int e = 0; e < 8; ++e) acc[e] = f32x2{0.f, 0.f};
#pragma unroll
      for (int i = 0; i < 12; ++i) {
        const f32x2 xv = *(LAS f32x2*)(L + XS + (12 * gr1 + i) * 8);
#pragma unroll
        for (int e = 0; e < 4; ++e) { acc[2 * e] += xv * bflo(wa[i][e]); acc[2 * e + 1] += xv * bfhi(wa[i][e]); }
      }
      {
        LAS f32x4* r = (LAS f32x4*)(L + R1 + gr1 * 576 + c1 * 32);
        r[0] = f32x4{acc[0].x, acc[1].x, acc[2].x, acc[3].x}; r[1] = f32x4{acc[4].x, acc[5].x, acc[6].x, acc[7].x};
        r[18] = f32x4{acc[0].y, acc[1].y, acc[2].y, acc[3].y}; r[19] = f32x4{acc[4].y, acc[5].y, acc[6].y, acc[7].y};
      }
    }
    LBAR();
    STAMP(1);
    if (tid < 144) {
      float v = bias;
      LAS float* r = (LAS float*)(L + R1) + tid;
#pragma unroll
      for (int i = 0; i < 48; ++i) v += r[i * 144];
      v = fmaxf(v, 0.f);
      *(LAS float*)(L + HS + (tid % 72) * 8 + (tid / 72) * 4) = v * v;
    }
    LBAR();
    {
      f32x2 acc[8];
#pragma unroll
      for (int e = 0; e < 8; ++e) acc[e] = f32x2{0.f, 0.f};
#pragma unroll
      for (int i = 0; i < 12; ++i) {
        const f32x2 hv = *(LAS f32x2*)(L + HS + (12 * gr2 + i) * 8);
#pragma unroll
        for (int e = 0; e < 4; ++e) { acc[2 * e] += hv * bflo(wb[i][e]); acc[2 * e + 1] += hv * bfhi(wb[i][e]); }
      }
      {
        LAS f32x4* r = (LAS f32x4*)(L + R2 + gr2 * 4608 + c2 * 64);
#pragma unroll
        for (int e = 0; e < 4; ++e) r[e] = f32x4{acc[2 * e].x, acc[2 * e].y, acc[2 * e + 1].x, acc[2 * e + 1].y};
      }
    }
    LBAR();
    for (int n = tid; n < NM; n += 512) {
      f32x2 v = {0.f, 0.f};
#pragma unroll
      for (int i = 0; i < 6; ++i) v += *(LAS f32x2*)(L + R2 + i * 4608 + n * 8);
      const v4u gr = {tagA, __float_as_uint(v.x), tagB, __float_as_uint(v.y)};
      __builtin_amdgcn_raw_buffer_store_b128(gr, rs, WS_POUT + ((n >> 3) * 128 + 64 + mi) * 128 + (n & 7) * 16, 0, AUX_SC1);
    }
    STAMP(2);
  }
}

extern "C" void kernel_launch(void* const* d_in, const int* in_sizes, int n_in, void* d_out, int out_size, void* d_ws, size_t ws_size,
                              hipStream_t stream) {
  fused_kernel<<<256, 512, 0, stream>>>((const u16*)d_in[0], (const u16*)d_in[1], (const u16*)d_in[2], (const u16*)d_in[3],
                                        (const u16*)d_in[4], (const u16*)d_in[5], (const u16*)d_in[7], (const u16*)d_in[8],
                                        (const u16*)d_in[9], (const u16*)d_in[10], (const u16*)d_in[11], (u16*)d_out, (char*)d_ws);
}
```

```cpp
#include <hip/hip_runtime.h>
#include <hip/hip_bf16.h>
#include <stdint.h>

#define GAS __attribute__((address_space(1)))
#define LAS __attribute__((address_space(3)))
#define DI __device__ __forceinline__
typedef unsigned short u16;
typedef unsigned long long u64;
typedef unsigned v4u __attribute__((ext_vector_type(4)));
typedef unsigned v2u __attribute__((ext_vector_type(2)));
typedef float f32x2 __attribute__((ext_vector_type(2)));
typedef float f32x4 __attribute__((ext_vector_type(4)));
typedef float f32x16 __attribute__((ext_vector_type(16)));
typedef short bf16x8 __attribute__((ext_vector_type(8)));
typedef short s16x4 __attribute__((ext_vector_type(4)));
typedef __bf16 bf2_t __attribute__((ext_vector_type(2)));

constexpr int NM = 576, NH = 32, ND = 128, NC = 512, NSK = 8192, NF = 4608;
constexpr int WS_EPOCH = 0;
constexpr int WS_QABS = 4096;
constexpr int WS_STAT = WS_QABS + 131072;
constexpr int WS_POUT = WS_STAT + 65536;
constexpr int WS_OPART = WS_POUT + 72 * 128 * 128;
constexpr int WS_STAMP = WS_OPART + 4194304;
constexpr int WS_BYTES = WS_STAMP + 256 * 256;
constexpr int AUX_SC1 = 16;
constexpr unsigned SPIN_CAP = 1u << 17;

#define T_GATHER 150
#define T_HEADB 300
#define T_MLP 380

#define STAMP(i) do {} while (0)
#define STAMPW(wv, i) do {} while (0)

DI unsigned fsw(unsigned r) { return ((r & 3u) << 2) | ((((r >> 3) ^ (r >> 2)) & 1u) << 1) | ((r >> 3) & 1u); }
DI float bflo(unsigned v) { return __uint_as_float(v << 16); }
DI float bfhi(unsigned v) { return __uint_as_float(v & 0xffff0000u); }
DI unsigned pk2(float a, float b) { f32x2 f = {a, b}; bf2_t r = __builtin_convertvector(f, bf2_t); return __builtin_bit_cast(unsigned, r); }
DI unsigned bf1(float a) { return pk2(a, 0.f) & 0xffffu; }
DI f32x4 mfma16(v4u a, v4u b, f32x4 c) { return __builtin_amdgcn_mfma_f32_16x16x32_bf16(__builtin_bit_cast(bf16x8, a), __builtin_bit_cast(bf16x8, b), c, 0, 0, 0); }
DI f32x16 mfma32(v4u a, v4u b, f32x16 c) { return __builtin_amdgcn_mfma_f32_32x32x16_bf16(__builtin_bit_cast(bf16x8, a), __builtin_bit_cast(bf16x8, b), c, 0, 0, 0); }
DI v2u trread(LAS char* p) { s16x4 r = __builtin_amdgcn_ds_read_tr16_b64_v4i16((LAS s16x4*)p); return __builtin_bit_cast(v2u, r); }
DI v4u trfrag(LAS char* p0, LAS char* p1) { v2u a = trread(p0), b = trread(p1); return v4u{a.x, a.y, b.x, b.y}; }
DI v4u lds128(LAS char* p) { return *(LAS v4u*)p; }
DI void dma16(const u16* g, LAS char* l) { __builtin_amdgcn_global_load_lds((const GAS unsigned*)g, (LAS unsigned*)l, 16, 0, 2); }
DI v4u gld(const u16* p) { return *(const GAS v4u*)p; }
DI v4u gldnt(const u16* p) { return __builtin_nontemporal_load((const GAS v4u*)p); }
template <int CTRL> DI float dppf(float x) { return __builtin_bit_cast(float, __builtin_amdgcn_mov_dpp(__builtin_bit_cast(int, x), CTRL, 0xf, 0xf, true)); }
template <int CTRL> DI unsigned dppu(unsigned x) { return (unsigned)__builtin_amdgcn_mov_dpp((int)x, CTRL, 0xf, 0xf, true); }
DI float sum32(float x) { auto t = __builtin_amdgcn_permlane32_swap(__float_as_uint(x), __float_as_uint(x), false, false); return __uint_as_float(t[0]) + __uint_as_float(t[1]); }
DI float max32(float x) { auto t = __builtin_amdgcn_permlane32_swap(__float_as_uint(x), __float_as_uint(x), false, false); return fmaxf(__uint_as_float(t[0]), __uint_as_float(t[1])); }
DI float sum16(float x) { auto t = __builtin_amdgcn_permlane16_swap(__float_as_uint(x), __float_as_uint(x), false, false); return __uint_as_float(t[0]) + __uint_as_float(t[1]); }
DI float max16(float x) { auto t = __builtin_amdgcn_permlane16_swap(__float_as_uint(x), __float_as_uint(x), false, false); return fmaxf(__uint_as_float(t[0]), __uint_as_float(t[1])); }
DI float wave_sum(float x) { x += dppf<0xB1>(x); x += dppf<0x4E>(x); x += dppf<0x141>(x); x += dppf<0x128>(x); x = sum16(x); return sum32(x); }
DI float wave_max(float x) { x = fmaxf(x, dppf<0xB1>(x)); x = fmaxf(x, dppf<0x4E>(x)); x = fmaxf(x, dppf<0x141>(x)); x = fmaxf(x, dppf<0x128>(x)); x = max16(x); return max32(x); }
DI void wait_until(u64 t) { while (__builtin_amdgcn_s_memrealtime() < t) __builtin_amdgcn_s_sleep(1); }
#define DRAIN() asm volatile("s_waitcnt vmcnt(0)" ::: "memory")
#define CBAR() asm volatile("" ::: "memory")
#define SCHED() __builtin_amdgcn_sched_barrier(0)
#define LBAR() asm volatile("s_waitcnt lgkmcnt(0)\n\ts_barrier" ::: "memory")

DI int tile_lane_off(int lane, int ld) { const int r8 = lane >> 3; return (8 * (r8 >> 2) + (r8 & 3)) * ld + 8 * ((lane & 7) ^ ((r8 >> 1) << 1)); }
DI int tile_tr_off(int lane, int ct) { const int r8 = 4 * ((lane >> 4) & 1) + ((lane >> 2) & 3); return (lane >> 5) * 1024 + r8 * 128 + ((ct ^ (r8 >> 1)) * 32) + (lane & 3) * 8; }

__global__ void __launch_bounds__(512, 2)
fused_kernel(const u16* __restrict__ x, const u16* __restrict__ ckv, const u16* __restrict__ wq, const u16* __restrict__ wuk,
             const u16* __restrict__ wuv, const u16* __restrict__ wo, const u16* __restrict__ w1, const u16* __restrict__ b1,
             const u16* __restrict__ w2, const u16* __restrict__ b2, const u16* __restrict__ gw, u16* __restrict__ out, char* ws) {
  __shared__ __attribute__((aligned(16))) char smem[163840];
  LAS char* L = (LAS char*)smem;
  const int tid = threadIdx.x, lane = tid & 63, w = __builtin_amdgcn_readfirstlane(tid >> 6);
  const int bid = blockIdx.x, xcd = bid & 7, kk = bid >> 3;
  const u64 t0 = __builtin_amdgcn_s_memrealtime();
  const __amdgpu_buffer_rsrc_t rs = __builtin_amdgcn_make_buffer_rsrc((void*)ws, 0, WS_BYTES, 0x00020000);
#define LOAD_EPOCH() (__hip_atomic_load((GAS u64*)(ws + WS_EPOCH), __ATOMIC_RELAXED, __HIP_MEMORY_SCOPE_AGENT) + 1ull)
  STAMP(0);
  const int g4 = lane >> 4, col = lane & 15;

  if (kk < 8) {
    const int h = xcd * 4 + (kk >> 1), j = kk & 1;
    constexpr int MISC = 147456, RED = MISC, CTXA = MISC + 8192, CTX2 = MISC + 10240, QA = MISC + 11264;
    {
      const u16* src = wq + h * ND + tile_lane_off(lane, 4096) + (16 * (w & 1) + 4 * (w >> 2)) * 4096 + 64 * ((w >> 1) & 1);
#pragma unroll
      for (int i = 0; i < 18; ++i) dma16(src + (size_t)i * 32 * 4096, L + (i * 8 + w) * 1024);
    }
    v4u uk[2][4];
#pragma unroll
    for (int T = 0; T < 2; ++T)
#pragma unroll
      for (int ks = 0; ks < 4; ++ks) uk[T][ks] = gldnt(wuk + (size_t)(256 * j + 32 * w + 16 * T + (lane >> 2)) * 4096 + h * ND + 32 * ks + 8 * (lane & 3));
    v4u xa[18];
#pragma unroll
    for (int i = 0; i < 18; ++i) xa[i] = gld(x + (col & 1) * NM + 32 * i + 8 * g4);
    const u64 epoch = LOAD_EPOCH();
    SCHED();
    DRAIN();
    {
      const int bi = (col * 4 + g4) * 4;
#pragma unroll
      for (int T = 0; T < 2; ++T)
#pragma unroll
        for (int ks = 0; ks < 4; ++ks)
#pragma unroll
          for (int e = 0; e < 4; ++e) uk[T][ks][e] = (unsigned)__builtin_amdgcn_ds_bpermute(bi, (int)uk[T][ks][e]);
    }
    __syncthreads();
    const unsigned tagA = (unsigned)epoch, tagB = (unsigned)(epoch >> 32);
    STAMP(1);
    {
      f32x4 acc = {0.f, 0.f, 0.f, 0.f};
      LAS char* p = L + (w >> 2) * 2048 + tile_tr_off(lane, w & 3);
#pragma unroll
      for (int i = 0; i < 18; ++i) acc = mfma16(xa[i], trfrag(p + i * 8192, p + i * 8192 + 4096), acc);
      const float v = (g4 & 1) ? acc[1] : acc[0];
      const unsigned hi = bf1(v);
      const unsigned o = (g4 & 2) ? bf1(v - bflo(hi)) : hi;
      *(LAS u16*)(L + QA + g4 * 256 + (16 * w + col) * 2) = (u16)o;
    }
    __syncthreads();
    {
      f32x4 a0 = {0.f, 0.f, 0.f, 0.f}, a1 = {0.f, 0.f, 0.f, 0.f};
#pragma unroll
      for (int ks = 0; ks < 4; ++ks) {
        const v4u qa = lds128(L + QA + (col & 3) * 256 + ks * 64 + g4 * 16);
        a0 = mfma16(qa, uk[0][ks], a0);
        a1 = mfma16(qa, uk[1][ks], a1);
      }
      const f32x4 a = (g4 & 2) ? a1 : a0;
      const float v = ((g4 & 1) ? (a[1] + a[3]) : (a[0] + a[2])) * (0.08838834764831845f * 1.4426950408889634f);
      const unsigned bits = bf1(v);
      const unsigned n1 = dppu<0x55>(bits), n2 = dppu<0xAA>(bits), n3 = dppu<0xFF>(bits);
      if ((lane & 3) == 0) {
        const v4u gr = {tagA, bits | (n1 << 16), tagB, n2 | (n3 << 16)};
        const int off = WS_QABS + (((g4 & 1) * 32 + h) * 128 + (256 * j + 32 * w + 16 * (g4 >> 1) + col) / 4) * 16;
        __builtin_amdgcn_raw_buffer_store_b128(gr, rs, off, 0, AUX_SC1);
      }
    }
    STAMP(2);
    wait_until(t0 + T_HEADB);
    __syncthreads();
    {
      const u16* src = wuv + (size_t)(256 * j) * 4096 + h * ND + tile_lane_off(lane, 4096) + (16 * (w & 1) + 4 * (w >> 2)) * 4096 + 64 * ((w >> 1) & 1);
#pragma unroll
      for (int i = 0; i < 8; ++i) dma16(src + (size_t)i * 32 * 4096, L + (i * 8 + w) * 1024);
      const u16* so = wo + (size_t)(h * ND) * NM + tile_lane_off(lane, NM) + (32 * (w >> 1) + 4 * (w & 1)) * NM;
#pragma unroll
      for (int c4 = 4; c4 < 9; ++c4)
#pragma unroll
        for (int gh = 0; gh < 2; ++gh) dma16(so + 16 * gh * NM + 64 * c4, L + ((c4 * 8 + w) * 2 + gh) * 1024);
    }
    asm volatile("s_waitcnt vmcnt(10)\n\ts_barrier" ::: "memory");
    v4u uv[8];
    {
      LAS char* p = L + (w >> 2) * 2048 + tile_tr_off(lane, w & 3);
#pragma unroll
      for (int i = 0; i < 8; ++i) {
        v2u a, b;
        asm volatile("ds_read_b64_tr_b16 %0, %2 offset:%3\n\tds_read_b64_tr_b16 %1, %2 offset:%4" : "=&v"(a), "=&v"(b) : "v"((unsigned)(size_t)p), "i"(i * 8192), "i"(i * 8192 + 4096) : "memory");
        uv[i] = v4u{a.x, a.y, b.x, b.y};
      }
      asm volatile("s_waitcnt lgkmcnt(0)" : "+v"(uv[0]), "+v"(uv[1]), "+v"(uv[2]), "+v"(uv[3]), "+v"(uv[4]), "+v"(uv[5]), "+v"(uv[6]), "+v"(uv[7]) :: "memory");
    }
    LBAR();
    {
      const u16* so = wo + (size_t)(h * ND) * NM + tile_lane_off(lane, NM) + (32 * (w >> 1) + 4 * (w & 1)) * NM;
#pragma unroll
      for (int c4 = 0; c4 < 4; ++c4)
#pragma unroll
        for (int gh = 0; gh < 2; ++gh) dma16(so + 16 * gh * NM + 64 * c4, L + ((c4 * 8 + w) * 2 + gh) * 1024);
    }
    DRAIN();
    STAMP(3);
    const int b = w >> 2, q4 = w & 3;
    float wgt;
    {
      const int off = WS_STAT + ((h * 2 + b) * 64 + lane) * 16;
      v4u g;
      for (unsigned spins = 0;; ++spins) {
        CBAR();
        g = __builtin_amdgcn_raw_buffer_load_b128(rs, off, 0, AUX_SC1);
        if (__all(g.x == tagA && g.z == tagB) || spins > SPIN_CAP) break;
        __builtin_amdgcn_s_sleep(2);
      }
      __builtin_amdgcn_fence(__ATOMIC_ACQUIRE, "wavefront");
      const float ms = __uint_as_float(g.y), ls = __uint_as_float(g.w);
      const float Mx = wave_max(ms);
      const float e = __builtin_amdgcn_exp2f(ms - Mx);
      const float Ls = wave_sum(e * ls);
      wgt = e / Ls;
    }
    STAMPW(0, 4);
    {
      v4u o[8];
      const int off = WS_OPART + ((((h * 2 + j) * 2 + b) * 64 + 16 * q4) * 512) + lane * 16;
#pragma unroll
      for (int i = 0; i < 8; ++i) o[i] = __builtin_amdgcn_raw_buffer_load_b128(rs, off + i * 1024, 0, AUX_SC1);
      float acc[8] = {0.f, 0.f, 0.f, 0.f, 0.f, 0.f, 0.f, 0.f};
#pragma unroll
      for (int i = 0; i < 8; ++i) {
        const float wa = __uint_as_float(__builtin_amdgcn_readlane(__float_as_uint(wgt), 16 * q4 + 2 * i));
        const float wb = __uint_as_float(__builtin_amdgcn_readlane(__float_as_uint(wgt), 16 * q4 + 2 * i + 1));
        const float ww = lane < 32 ? wa : wb;
        acc[0] += ww * bflo(o[i].x); acc[1] += ww * bfhi(o[i].x);
        acc[2] += ww * bflo(o[i].y); acc[3] += ww * bfhi(o[i].y);
        acc[4] += ww * bflo(o[i].z); acc[5] += ww * bfhi(o[i].z);
        acc[6] += ww * bflo(o[i].w); acc[7] += ww * bfhi(o[i].w);
      }
#pragma unroll
      for (int e = 0; e < 8; ++e) acc[e] = sum32(acc[e]);
      const f32x4 v = lane < 32 ? f32x4{acc[0], acc[1], acc[2], acc[3]} : f32x4{acc[4], acc[5], acc[6], acc[7]};
      *(LAS f32x4*)(L + RED + w * 1024 + (lane & 31) * 32 + (lane >> 5) * 16) = v;
    }
    __syncthreads();
    {
      const int bb = tid >> 8, c = tid & 255;
      LAS float* r = (LAS float*)(L + RED + bb * 4096) + c;
      const float v = (r[0] + r[256]) + (r[512] + r[768]);
      const unsigned hi = bf1(v);
      *(LAS u16*)(L + CTXA + bb * 512 + c * 2) = (u16)hi;
      *(LAS u16*)(L + CTXA + (2 + bb) * 512 + c * 2) = (u16)bf1(v - bflo(hi));
    }
    __syncthreads();
    {
      f32x4 acc = {0.f, 0.f, 0.f, 0.f};
#pragma unroll
      for (int i = 0; i < 8; ++i) acc = mfma16(lds128(L + CTXA + (col & 3) * 512 + i * 64 + g4 * 16), uv[i], acc);
      const float v = (g4 & 1) ? (acc[1] + acc[3]) : (acc[0] + acc[2]);
      const unsigned hi = bf1(v);
      const unsigned o = (g4 & 2) ? bf1(v - bflo(hi)) : hi;
      *(LAS u16*)(L + CTX2 + g4 * 256 + (16 * w + col) * 2) = (u16)o;
    }
    __syncthreads();
    {
      v4u ca[4];
#pragma unroll
      for (int ks = 0; ks < 4; ++ks) ca[ks] = lds128(L + CTX2 + (col & 3) * 256 + ks * 64 + g4 * 16);
      f32x4 acc[5];
#pragma unroll
      for (int i = 0; i < 5; ++i) {
        acc[i] = f32x4{0.f, 0.f, 0.f, 0.f};
        if (i < 4 || w < 4) {
          const int ct = w + 8 * i;
          LAS char* p = L + (ct >> 2) * 16384 + tile_tr_off(lane, w & 3);
#pragma unroll
          for (int ks = 0; ks < 4; ++ks) acc[i] = mfma16(ca[ks], trfrag(p + ks * 4096, p + ks * 4096 + 2048), acc[i]);
        }
      }
      const f32x4 a = g4 == 0 ? acc[0] : g4 == 1 ? acc[1] : g4 == 2 ? acc[2] : acc[3];
      const int p = h * 2 + j;
      {
        const int ct = w + 8 * g4;
        const v4u gr = {tagA, __float_as_uint(a[0] + a[2]), tagB, __float_as_uint(a[1] + a[3])};
        __builtin_amdgcn_raw_buffer_store_b128(gr, rs, WS_POUT + ((2 * ct + (col >> 3)) * 128 + p) * 128 + (col & 7) * 16, 0, AUX_SC1);
      }
      if (w < 4 && g4 == 0) {
        const int ct = w + 32;
        const v4u gr = {tagA, __float_as_uint(acc[4][0] + acc[4][2]), tagB, __float_as_uint(acc[4][1] + acc[4][3])};
        __builtin_amdgcn_raw_buffer_store_b128(gr, rs, WS_POUT + ((2 * ct + (col >> 3)) * 128 + p) * 128 + (col & 7) * 16, 0, AUX_SC1);
      }
    }
    STAMP(5);
    __builtin_amdgcn_endpgm();
  } else if (kk < 24) {
    const int a = xcd * 16 + (kk - 8), b = a >> 6, s = a & 63;
    constexpr int QO = 131072, XCH = QO, PB = QO + 16384, MX = QO + 24576, LS = QO + 25600;
    if (w < 4) {
      const u16* src = ckv + ((size_t)b * NSK + 128 * s + 32 * w) * NC;
#pragma unroll
      for (int i = 0; i < 32; ++i) dma16(src + i * NC + ((lane ^ fsw(i & 15)) * 8), L + (32 * w + i) * 1024);
    }
    SCHED();
    const u64 epoch = LOAD_EPOCH();
    const unsigned tagA = (unsigned)epoch, tagB = (unsigned)(epoch >> 32);
    if (w < 4) {
      DRAIN();
      STAMPW(0, 1);
    } else {
      wait_until(t0 + T_GATHER);
      const int gl = ((w - 4) & 1) * 64 + lane, h0 = (w - 4) >> 1;
      const int off = WS_QABS + ((b * 32 + h0) * 128 + gl) * 16;
      v4u g[16];
      for (unsigned spins = 0;; ++spins) {
        CBAR();
        bool ok = true;
#pragma unroll
        for (int u = 0; u < 16; ++u) g[u] = __builtin_amdgcn_raw_buffer_load_b128(rs, off + u * 4096, 0, AUX_SC1);
#pragma unroll
        for (int u = 0; u < 16; ++u) ok &= (g[u].x == tagA) & (g[u].z == tagB);
        if (__all(ok) || spins > SPIN_CAP) break;
        __builtin_amdgcn_s_sleep(4);
      }
#pragma unroll
      for (int u = 0; u < 16; ++u) {
        const int hd = 2 * u + h0;
        *(LAS v2u*)(L + QO + hd * 1024 + (((gl >> 1) ^ fsw(hd & 15)) << 4) + (gl & 1) * 8) = v2u{g[u].y, g[u].w};
      }
      STAMPW(4, 2);
    }
    __syncthreads();
    STAMP(3);
    const int r32 = lane & 31, hf = lane >> 5, kt = w & 3, ch = w >> 2;
    const unsigned fr = fsw(r32 & 15);
    f32x16 S;
#pragma unroll
    for (int i = 0; i < 16; ++i) S[i] = 0.f;
    {
      const unsigned ob = r32 * 1024 + ch * 512 + ((hf ^ fr) << 4);
#pragma unroll
      for (int st = 0; st < 16; ++st) {
        const unsigned o = (ob ^ ((st & 7) << 5)) + (st >> 3) * 256;
        S = mfma32(lds128(L + kt * 32768 + o), lds128(L + QO + o), S);
      }
    }
    __syncthreads();
    {
      LAS f32x4* xw = (LAS f32x4*)(L + XCH + w * 2048 + lane * 16);
      if (ch == 0) { xw[0] = f32x4{S[8], S[9], S[10], S[11]}; xw[64] = f32x4{S[12], S[13], S[14], S[15]}; }
      else         { xw[0] = f32x4{S[0], S[1], S[2], S[3]};   xw[64] = f32x4{S[4], S[5], S[6], S[7]}; }
    }
    __syncthreads();
    float sv[8];
    {
      LAS f32x4* xr = (LAS f32x4*)(L + XCH + (w ^ 4) * 2048 + lane * 16);
      const f32x4 p0 = xr[0], p1 = xr[64];
#pragma unroll
      for (int e = 0; e < 4; ++e) {
        sv[e] = (ch == 0 ? S[e] : S[8 + e]) + p0[e];
        sv[4 + e] = (ch == 0 ? S[4 + e] : S[12 + e]) + p1[e];
      }
      float m = fmaxf(fmaxf(fmaxf(sv[0], sv[1]), fmaxf(sv[2], sv[3])), fmaxf(fmaxf(sv[4], sv[5]), fmaxf(sv[6], sv[7])));
      m = max32(m);
      if (lane < 32) *(LAS float*)(L + MX + lane * 32 + w * 4) = m;
    }
    __syncthreads();
    float Mx;
    {
      const f32x4 m0 = *(LAS f32x4*)(L + MX + r32 * 32), m1 = *(LAS f32x4*)(L + MX + r32 * 32 + 16);
      Mx = fmaxf(fmaxf(fmaxf(m0[0], m0[1]), fmaxf(m0[2], m0[3])), fmaxf(fmaxf(m1[0], m1[1]), fmaxf(m1[2], m1[3])));
      float l = 0.f;
#pragma unroll
      for (int e = 0; e < 8; ++e) { sv[e] = __builtin_amdgcn_exp2f(sv[e] - Mx); l += sv[e]; }
      l = sum32(l);
      if (lane < 32) *(LAS float*)(L + LS + lane * 32 + w * 4) = l;
#pragma unroll
      for (int i = 0; i < 2; ++i)
        *(LAS v2u*)(L + PB + r32 * 256 + (((4 * kt + 2 * ch + i) ^ fr) << 4) + hf * 8) = v2u{pk2(sv[4 * i], sv[4 * i + 1]), pk2(sv[4 * i + 2], sv[4 * i + 3])};
    }
    __syncthreads();
    STAMP(4);
    float Ls;
    f32x16 O0, O1;
#pragma unroll
    for (int i = 0; i < 16; ++i) { O0[i] = 0.f; O1[i] = 0.f; }
    {
      const f32x4 l0 = *(LAS f32x4*)(L + LS + r32 * 32), l1 = *(LAS f32x4*)(L + LS + r32 * 32 + 16);
      Ls = ((l0[0] + l0[1]) + (l0[2] + l0[3])) + ((l1[0] + l1[1]) + (l1[2] + l1[3]));
      const unsigned pb = PB + r32 * 256 + ((hf ^ fr) << 4);
      const int blk = (lane >> 4) & 1, q = (lane >> 2) & 3, p = lane & 3;
      unsigned va[2][2];
#pragma unroll
      for (int t = 0; t < 2; ++t)
#pragma unroll
        for (int cc = 0; cc < 2; ++cc)
          va[t][cc] = (8 * hf + 4 * t + q) * 1024 + (((8 * w + 4 * cc + 2 * blk + (p >> 1)) ^ (4 * q + 2 * (hf ^ t) + hf)) << 4) + 8 * (p & 1);
#pragma unroll
      for (int ks = 0; ks < 8; ++ks) {
        const v4u pf = lds128(L + (pb ^ (ks << 5)));
        O0 = mfma32(trfrag(L + va[0][0] + ks * 16384, L + va[1][0] + ks * 16384), pf, O0);
        O1 = mfma32(trfrag(L + va[0][1] + ks * 16384, L + va[1][1] + ks * 16384), pf, O1);
      }
    }
    __syncthreads();
#pragma unroll
    for (int gq = 0; gq < 4; ++gq) {
      *(LAS v2u*)(L + QO + r32 * 1024 + (((8 * w + gq) ^ fr) << 4) + 8 * hf) = v2u{pk2(O0[4 * gq], O0[4 * gq + 1]), pk2(O0[4 * gq + 2], O0[4 * gq + 3])};
      *(LAS v2u*)(L + QO + r32 * 1024 + (((8 * w + 4 + gq) ^ fr) << 4) + 8 * hf) = v2u{pk2(O1[4 * gq], O1[4 * gq + 1]), pk2(O1[4 * gq + 2], O1[4 * gq + 3])};
    }
    __syncthreads();
#pragma unroll
    for (int i = 0; i < 4; ++i) {
      const int hd = 4 * w + i;
      const v4u v = lds128(L + QO + hd * 1024 + lane * 16);
      const int lc = lane ^ fsw(hd & 15);
      __builtin_amdgcn_raw_buffer_store_b128(v, rs, WS_OPART + ((((hd * 2 + (lc >> 5)) * 2 + b) * 64 + s) * 512) + (lc & 31) * 16, 0, AUX_SC1);
    }
    STAMP(5);
    DRAIN();
    if ((lane >> 2) == w) {
      const v4u gr = {tagA, __float_as_uint(Mx), tagB, __float_as_uint(Ls)};
      __builtin_amdgcn_raw_buffer_store_b128(gr, rs, WS_STAT + ((lane * 2 + b) * 64 + s) * 16, 0, AUX_SC1);
    }
    STAMP(6);
    if ((a & 15) < 9) {
      const int r = (a >> 4) * 9 + (a & 15);
      float g0, g1;
      {
        float d0 = 0.f, d1 = 0.f;
#pragma unroll
        for (int i = 0; i < 2; ++i) {
          const int c = lane + 64 * i;
          if (c < 72) {
            const v4u xa = gld(x + c * 8), xb = gld(x + NM + c * 8), gg = gld(gw + c * 8);
#pragma unroll
            for (int e = 0; e < 4; ++e) {
              d0 += bflo(xa[e]) * bflo(gg[e]) + bfhi(xa[e]) * bfhi(gg[e]);
              d1 += bflo(xb[e]) * bflo(gg[e]) + bfhi(xb[e]) * bfhi(gg[e]);
            }
          }
        }
        d0 = wave_sum(d0); d1 = wave_sum(d1);
        g0 = 1.f / (1.f + __expf(-d0)); g1 = 1.f / (1.f + __expf(-d1));
      }
      const float bias = tid < 16 ? bflo((unsigned)b2[8 * r + (tid & 7)]) : 0.f;
      const int off = WS_POUT + r * 16384 + tid * 16;
      v4u ga, gb;
      for (unsigned spins = 0;; ++spins) {
        CBAR();
        ga = __builtin_amdgcn_raw_buffer_load_b128(rs, off, 0, AUX_SC1);
        gb = __builtin_amdgcn_raw_buffer_load_b128(rs, off + 8192, 0, AUX_SC1);
        if (__all(ga.x == tagA && ga.z == tagB && gb.x == tagA && gb.z == tagB) || spins > SPIN_CAP) break;
        __builtin_amdgcn_s_sleep(2);
      }
      STAMP(7);
      float v0 = g0 * __uint_as_float(ga.y) + (1.f - g0) * __uint_as_float(gb.y);
      float v1 = g1 * __uint_as_float(ga.w) + (1.f - g1) * __uint_as_float(gb.w);
      v0 += dppf<0x128>(v0); v1 += dppf<0x128>(v1);
      v0 = sum32(sum16(v0)); v1 = sum32(sum16(v1));
      if (lane < 8) { *(LAS float*)(L + (lane * 8 + w) * 4) = v0; *(LAS float*)(L + 256 + (lane * 8 + w) * 4) = v1; }
      __syncthreads();
      if (tid < 16) {
        const f32x4 p0 = *(LAS f32x4*)(L + tid * 32), p1 = *(LAS f32x4*)(L + tid * 32 + 16);
        const float v = ((p0[0] + p0[1]) + (p0[2] + p0[3])) + ((p1[0] + p1[1]) + (p1[2] + p1[3])) + (1.f - (tid < 8 ? g0 : g1)) * bias;
        out[(tid >> 3) * NM + 8 * r + (tid & 7)] = (u16)bf1(v);
      }
      if (r == 0 && tid == 0) __hip_atomic_store((GAS u64*)(ws + WS_EPOCH), epoch, __ATOMIC_RELAXED, __HIP_MEMORY_SCOPE_AGENT);
      STAMP(8);
      if (r == 0) { u64 tt = *(GAS u64*)(ws + WS_STAMP); wait_until(tt + 10000); }
    }
    __builtin_amdgcn_endpgm();
  } else {
    const int mi = xcd * 8 + (kk - 24), f0 = 72 * mi;
    constexpr int XS = 0, R1 = 8192, HS = R1 + 48 * 144 * 4, R2 = HS + 1024;
    const u64 epoch = LOAD_EPOCH();
    const unsigned tagA = (unsigned)epoch, tagB = (unsigned)(epoch >> 32);
    for (int i = tid; i < NM; i += 512) *(LAS f32x2*)(L + XS + i * 8) = f32x2{bflo((unsigned)x[i]), bflo((unsigned)x[NM + i])};
    const float bias = tid < 144 ? bflo((unsigned)b1[f0 + (tid % 72)]) : 0.f;
    wait_until(t0 + T_MLP);
    const int tt = tid < 432 ? tid : 431;
    const int c1 = tt % 9, gr1 = tt / 9, c2 = tt % 72, gr2 = tt / 72;
    v4u wa[12], wb[12];
#pragma unroll
    for (int i = 0; i < 12; ++i) wa[i] = gldnt(w1 + (size_t)(12 * gr1 + i) * NF + f0 + 8 * c1);
#pragma unroll
    for (int i = 0; i < 12; ++i) wb[i] = gldnt(w2 + (size_t)(f0 + 12 * gr2 + i) * NM + 8 * c2);
    SCHED();
    LBAR();
    {
      f32x2 acc[8];
#pragma unroll
      for (int e = 0; e < 8; ++e) acc[e] = f32x2{0.f, 0.f};
#pragma unroll
      for (int i = 0; i < 12; ++i) {
        const f32x2 xv = *(LAS f32x2*)(L + XS + (12 * gr1 + i) * 8);
#pragma unroll
        for (int e = 0; e < 4; ++e) { acc[2 * e] += xv * bflo(wa[i][e]); acc[2 * e + 1] += xv * bfhi(wa[i][e]); }
      }
      {
        LAS f32x4* r = (LAS f32x4*)(L + R1 + gr1 * 576 + c1 * 32);
        r[0] = f32x4{acc[0].x, acc[1].x, acc[2].x, acc[3].x}; r[1] = f32x4{acc[4].x, acc[5].x, acc[6].x, acc[7].x};
        r[18] = f32x4{acc[0].y, acc[1].y, acc[2].y, acc[3].y}; r[19] = f32x4{acc[4].y, acc[5].y, acc[6].y, acc[7].y};
      }
    }
    LBAR();
    STAMP(1);
    if (tid < 144) {
      float v = bias;
      LAS float* r = (LAS float*)(L + R1) + tid;
#pragma unroll
      for (int i = 0; i < 48; ++i) v += r[i * 144];
      v = fmaxf(v, 0.f);
      *(LAS float*)(L + HS + (tid % 72) * 8 + (tid / 72) * 4) = v * v;
    }
    LBAR();
    {
      f32x2 acc[8];
#pragma unroll
      for (int e = 0; e < 8; ++e) acc[e] = f32x2{0.f, 0.f};
#pragma unroll
      for (int i = 0; i < 12; ++i) {
        const f32x2 hv = *(LAS f32x2*)(L + HS + (12 * gr2 + i) * 8);
#pragma unroll
        for (int e = 0; e < 4; ++e) { acc[2 * e] += hv * bflo(wb[i][e]); acc[2 * e + 1] += hv * bfhi(wb[i][e]); }
      }
      {
        LAS f32x4* r = (LAS f32x4*)(L + R2 + gr2 * 4608 + c2 * 64);
#pragma unroll
        for (int e = 0; e < 4; ++e) r[e] = f32x4{acc[2 * e].x, acc[2 * e].y, acc[2 * e + 1].x, acc[2 * e + 1].y};
      }
    }
    LBAR();
    for (int n = tid; n < NM; n += 512) {
      f32x2 v = {0.f, 0.f};
#pragma unroll
      for (int i = 0; i < 6; ++i) v += *(LAS f32x2*)(L + R2 + i * 4608 + n * 8);
      const v4u gr = {tagA, __float_as_uint(v.x), tagB, __float_as_uint(v.y)};
      __builtin_amdgcn_raw_buffer_store_b128(gr, rs, WS_POUT + ((n >> 3) * 128 + 64 + mi) * 128 + (n & 7) * 16, 0, AUX_SC1);
    }
    STAMP(2);
  }
}

__global__ void probe_tiny(char* ws) { if (threadIdx.x == 0 && blockIdx.x == 0) *(GAS u64*)(ws + WS_STAMP) = __builtin_amdgcn_s_memrealtime(); }
__global__ void __launch_bounds__(512, 2) probe_big(char* ws) {
  __shared__ char sm[163840];
  u64 t = __builtin_amdgcn_s_memrealtime();
  if (threadIdx.x == 0 && blockIdx.x == 0) *(GAS u64*)(ws + WS_STAMP) = t;
  if (t == 12345) { sm[threadIdx.x] = 1; __syncthreads(); ws[threadIdx.x] = sm[threadIdx.x ^ 1]; }
}
__global__ void tail_tiny(char* ws) { u64 t = *(GAS u64*)(ws + WS_STAMP); wait_until(t + 10000); }
extern "C" void kernel_launch(void* const* d_in, const int* in_sizes, int n_in, void* d_out, int out_size, void* d_ws, size_t ws_size,
                              hipStream_t stream) {
  probe_big<<<256, 512, 0, stream>>>((char*)d_ws);
  fused_kernel<<<256, 512, 0, stream>>>((const u16*)d_in[0], (const u16*)d_in[1], (const u16*)d_in[2], (const u16*)d_in[3],
                                        (const u16*)d_in[4], (const u16*)d_in[5], (const u16*)d_in[7], (const u16*)d_in[8],
                                        (const u16*)d_in[9], (const u16*)d_in[10], (const u16*)d_in[11], (u16*)d_out, (char*)d_ws);
}
```

```cpp
#include <hip/hip_runtime.h>
#include <hip/hip_bf16.h>
#include <stdint.h>

#define GAS __attribute__((address_space(1)))
#define LAS __attribute__((address_space(3)))
#define DI __device__ __forceinline__
typedef unsigned short u16;
typedef unsigned long long u64;
typedef unsigned v4u __attribute__((ext_vector_type(4)));
typedef unsigned v2u __attribute__((ext_vector_type(2)));
typedef float f32x2 __attribute__((ext_vector_type(2)));
typedef float f32x4 __attribute__((ext_vector_type(4)));
typedef float f32x16 __attribute__((ext_vector_type(16)));
typedef short bf16x8 __attribute__((ext_vector_type(8)));
typedef short s16x4 __attribute__((ext_vector_type(4)));
typedef __bf16 bf2_t __attribute__((ext_vector_type(2)));

constexpr int NM = 576, NH = 32, ND = 128, NC = 512, NSK = 8192, NF = 4608;
constexpr int WS_EPOCH = 0;
constexpr int WS_HX = 4096;
constexpr int WS_QX = WS_HX + 73728;
constexpr int WS_CX = WS_QX + 262144;
constexpr int WS_QABS = WS_CX + 262144;
constexpr int WS_STAT = WS_QABS + 131072;
constexpr int WS_POUT = WS_STAT + 524288;
constexpr int WS_OPART = WS_POUT + 72 * 128 * 128;
constexpr int WS_STAMP = WS_OPART + 4194304;
constexpr int WS_BYTES = WS_STAMP + 2 * 256 * 256;
constexpr int AUX_SC1 = 16;
constexpr unsigned SPIN_CAP = 1u << 17;

#define CKV_N1 0
#define CKV_WIN 2
#define CKV_WIN2 16
#define PRETOUCH 0
#define HO0_MODE 2
#define T_CKV 160
#define POLL_SLEEP 2
#define T_STATS 600
#define T_HEADB 0
#define T_RED 800

#define STAMP(i) do {} while (0)
#define STAMPW(wv, i) do {} while (0)
#define STAMP_FLUSH() do {} while (0)
#define CYC(i) do {} while (0)

DI unsigned fsw(unsigned r) { return ((r & 3u) << 2) | ((((r >> 3) ^ (r >> 2)) & 1u) << 1) | ((r >> 3) & 1u); }
DI float bflo(unsigned v) { return __uint_as_float(v << 16); }
DI float bfhi(unsigned v) { return __uint_as_float(v & 0xffff0000u); }
DI unsigned pk2(float a, float b) { f32x2 f = {a, b}; bf2_t r = __builtin_convertvector(f, bf2_t); return __builtin_bit_cast(unsigned, r); }
DI unsigned bf1(float a) { return pk2(a, 0.f) & 0xffffu; }
DI f32x4 mfma16(v4u a, v4u b, f32x4 c) { return __builtin_amdgcn_mfma_f32_16x16x32_bf16(__builtin_bit_cast(bf16x8, a), __builtin_bit_cast(bf16x8, b), c, 0, 0, 0); }
DI f32x16 mfma32(v4u a, v4u b, f32x16 c) { return __builtin_amdgcn_mfma_f32_32x32x16_bf16(__builtin_bit_cast(bf16x8, a), __builtin_bit_cast(bf16x8, b), c, 0, 0, 0); }
DI v2u trread(LAS char* p) { s16x4 r = __builtin_amdgcn_ds_read_tr16_b64_v4i16((LAS s16x4*)p); return __builtin_bit_cast(v2u, r); }
DI v4u trfrag(LAS char* p0, LAS char* p1) { v2u a = trread(p0), b = trread(p1); return v4u{a.x, a.y, b.x, b.y}; }
DI v4u lds128(LAS char* p) { return *(LAS v4u*)p; }
DI void dma16(const u16* g, LAS char* l) { __builtin_amdgcn_global_load_lds((const GAS unsigned*)g, (LAS unsigned*)l, 16, 0, 2); }
DI v4u gld(const u16* p) { return *(const GAS v4u*)p; }
DI v4u gldnt(const u16* p) { return __builtin_nontemporal_load((const GAS v4u*)p); }
DI v2u gldnt8(const u16* p) { return __builtin_nontemporal_load((const GAS v2u*)p); }
template <int CTRL> DI float dppf(float x) { return __builtin_bit_cast(float, __builtin_amdgcn_mov_dpp(__builtin_bit_cast(int, x), CTRL, 0xf, 0xf, true)); }
template <int CTRL> DI unsigned dppu(unsigned x) { return (unsigned)__builtin_amdgcn_mov_dpp((int)x, CTRL, 0xf, 0xf, true); }
DI float sum32(float x) { auto t = __builtin_amdgcn_permlane32_swap(__float_as_uint(x), __float_as_uint(x), false, false); return __uint_as_float(t[0]) + __uint_as_float(t[1]); }
DI float max32(float x) { auto t = __builtin_amdgcn_permlane32_swap(__float_as_uint(x), __float_as_uint(x), false, false); return fmaxf(__uint_as_float(t[0]), __uint_as_float(t[1])); }
DI float sum16(float x) { auto t = __builtin_amdgcn_permlane16_swap(__float_as_uint(x), __float_as_uint(x), false, false); return __uint_as_float(t[0]) + __uint_as_float(t[1]); }
DI float max16(float x) { auto t = __builtin_amdgcn_permlane16_swap(__float_as_uint(x), __float_as_uint(x), false, false); return fmaxf(__uint_as_float(t[0]), __uint_as_float(t[1])); }
DI float wave_sum(float x) { x += dppf<0xB1>(x); x += dppf<0x4E>(x); x += dppf<0x141>(x); x += dppf<0x128>(x); x = sum16(x); return sum32(x); }
DI float wave_max(float x) { x = fmaxf(x, dppf<0xB1>(x)); x = fmaxf(x, dppf<0x4E>(x)); x = fmaxf(x, dppf<0x141>(x)); x = fmaxf(x, dppf<0x128>(x)); x = max16(x); return max32(x); }
DI void wait_until(u64 t) { while (__builtin_amdgcn_s_memrealtime() < t) __builtin_amdgcn_s_sleep(1); }
#define DRAIN() asm volatile("s_waitcnt vmcnt(0)" ::: "memory")
#define CBAR() asm volatile("" ::: "memory")
#define SCHED() __builtin_amdgcn_sched_barrier(0)
#define LBAR() asm volatile("s_waitcnt lgkmcnt(0)\n\ts_barrier" ::: "memory")
#define DMA16(gp, ldsoff) do { unsigned keep_; asm volatile("s_mov_b32 %0, m0\n\ts_mov_b32 m0, %2\n\ts_nop 0\n\tglobal_load_lds_dwordx4 %1, off nt\n\ts_mov_b32 m0, %0" : "=&s"(keep_) : "v"(gp), "s"((unsigned)(size_t)L + (unsigned)(ldsoff)) : "memory"); } while (0)
#define TOUCH(dst, off) asm volatile("buffer_load_dword %0, %1, %2, 0 offen sc1" : "=&v"(dst) : "v"(off), "s"(rs) : "memory")
#define LDG(off) __builtin_amdgcn_raw_buffer_load_b128(rs, (off), 0, AUX_SC1)
#define STG(v, off) __builtin_amdgcn_raw_buffer_store_b128((v), rs, (off), 0, AUX_SC1)
#define TAGOK(g) (((g).x == tagA) & ((g).z == tagB))

DI int tile_lane_off(int lane, int ld) { const int r8 = lane >> 3; return (8 * (r8 >> 2) + (r8 & 3)) * ld + 8 * ((lane & 7) ^ ((r8 >> 1) << 1)); }
DI int tile_tr_off(int lane, int ct) { const int r8 = 4 * ((lane >> 4) & 1) + ((lane >> 2) & 3); return (lane >> 5) * 1024 + r8 * 128 + ((ct ^ (r8 >> 1)) * 32) + (lane & 3) * 8; }
DI void put_hilo(LAS char* base, int stride, int d, float v0, float v1) {
  const unsigned h = pk2(v0, v1);
  const unsigned l = pk2(v0 - bflo(h), v1 - bfhi(h));
  *(LAS u16*)(base + d * 2) = (u16)h;
  *(LAS u16*)(base + stride + d * 2) = (u16)(h >> 16);
  *(LAS u16*)(base + 2 * stride + d * 2) = (u16)l;
  *(LAS u16*)(base + 3 * stride + d * 2) = (u16)(l >> 16);
}

__global__ void __launch_bounds__(512, 2)
fused_kernel(const u16* __restrict__ x, const u16* __restrict__ ckv, const u16* __restrict__ wq, const u16* __restrict__ wuk,
             const u16* __restrict__ wuv, const u16* __restrict__ wo, const u16* __restrict__ w1, const u16* __restrict__ b1,
             const u16* __restrict__ w2, const u16* __restrict__ b2, const u16* __restrict__ gw, u16* __restrict__ out, char* ws) {
  __shared__ __attribute__((aligned(16))) char smem[163840];
  LAS char* L = (LAS char*)smem;
  const int tid = threadIdx.x, lane = tid & 63, w = __builtin_amdgcn_readfirstlane(tid >> 6);
  const int bid = blockIdx.x, xcd = bid & 7, kk = bid >> 3;
  const u64 t0 = __builtin_amdgcn_s_memrealtime();
  const __amdgpu_buffer_rsrc_t rs = __builtin_amdgcn_make_buffer_rsrc((void*)ws, 0, WS_BYTES, 0x00020000);
#define LOAD_EPOCH() (__hip_atomic_load((GAS u64*)(ws + WS_EPOCH), __ATOMIC_RELAXED, __HIP_MEMORY_SCOPE_AGENT) + 1ull)
  STAMP(0);
  const int g4 = lane >> 4, col = lane & 15;

  if (kk < 16) {
    const int h = xcd * 4 + (kk >> 2), j = kk & 3;
    constexpr int WQ = 0, WUK = 40960, W1 = 69632, W2 = 69632, W2X = 143360, WUV = 0, WO = 32768;
    constexpr int RED = 152576, CTXA = RED + 4096, QA = CTXA + 1024, PRIV = QA + 1024, HR = PRIV + 2048;
    const int ks0 = j < 2 ? 5 * j : 4 * j + 2;
    v4u xa[5];
    u64 epoch;
    {
      const u16* xp = x + (col & 1) * NM + 32 * ks0 + 8 * g4;
      const u16* xp4 = j < 2 ? xp + 128 : xp;
      asm volatile("global_load_dwordx4 %0, %6, off\n\tglobal_load_dwordx4 %1, %6, off offset:64\n\tglobal_load_dwordx4 %2, %6, off offset:128\n\t"
                   "global_load_dwordx4 %3, %6, off offset:192\n\tglobal_load_dwordx4 %4, %7, off\n\tglobal_load_dwordx2 %5, %8, off sc1"
                   : "=&v"(xa[0]), "=&v"(xa[1]), "=&v"(xa[2]), "=&v"(xa[3]), "=&v"(xa[4]), "=&v"(epoch) : "v"(xp), "v"(xp4), "v"(ws + WS_EPOCH) : "memory");
    }
    {
      const u16* src = wq + (size_t)(32 * ks0) * 4096 + h * ND + tile_lane_off(lane, 4096) + (16 * (w & 1) + 4 * (w >> 2)) * 4096 + 64 * ((w >> 1) & 1);
#pragma unroll
      for (int i = 0; i < 5; ++i)
        if (i < 4 || j < 2) DMA16(src + (size_t)i * 32 * 4096, WQ + (i * 8 + w) * 1024);
#pragma unroll
      for (int i = 0; i < 4; ++i) {
        const int row = 16 * w + 4 * i + g4;
        DMA16(wuk + (size_t)(128 * j + row) * 4096 + h * ND + 8 * (col ^ fsw(4 * i + g4)), WUK + (4 * w + i) * 1024);
      }
    }
    asm volatile("s_waitcnt vmcnt(4)\n\ts_barrier" : "+v"(xa[0]), "+v"(xa[1]), "+v"(xa[2]), "+v"(xa[3]), "+v"(xa[4]), "+v"(epoch) :: "memory");
    epoch += 1ull;
    const unsigned tagA = (unsigned)epoch, tagB = (unsigned)(epoch >> 32);
    STAMP(1);
    {
      f32x4 acc = {0.f, 0.f, 0.f, 0.f};
      LAS char* p = L + WQ + (w >> 2) * 2048 + tile_tr_off(lane, w & 3);
      v4u bq[5];
#pragma unroll
      for (int i = 0; i < 4; ++i) bq[i] = trfrag(p + i * 8192, p + i * 8192 + 4096);
      if (j < 2) bq[4] = trfrag(p + 4 * 8192, p + 4 * 8192 + 4096);
#pragma unroll
      for (int i = 0; i < 4; ++i) acc = mfma16(xa[i], bq[i], acc);
      if (j < 2) acc = mfma16(xa[4], bq[4], acc);
      if (g4 == 0) { const v4u gr = {tagA, __float_as_uint(acc[0]), tagB, __float_as_uint(acc[1])}; STG(gr, WS_QX + ((h * 4 + j) * 128 + 16 * w + col) * 16); }
    }
    STAMP(2);
    DRAIN(); __builtin_amdgcn_s_sleep(8);
    LAS char* pv = L + PRIV + w * 256;
    {
      v4u ga;
      const int off = WS_QX + ((h * 4 + g4) * 128 + 16 * w + col) * 16;
      for (unsigned spins = 0;; ++spins) {
        CBAR();
        ga = LDG(off);
        if (__all(TAGOK(ga)) || spins > SPIN_CAP) break;
        __builtin_amdgcn_s_sleep(1);
      }
      STAMP(3);
      v4u uk[4];
#pragma unroll
      for (int ks = 0; ks < 4; ++ks) uk[ks] = lds128(L + WUK + (16 * w + col) * 256 + (((4 * ks + g4) ^ fsw(col)) << 4));
      const float q0 = sum32(sum16(__uint_as_float(ga.y))), q1 = sum32(sum16(__uint_as_float(ga.w)));
      const float qv = (g4 & 1) ? q1 : q0;
      const unsigned hi = bf1(qv);
      *(LAS u16*)(L + QA + g4 * 256 + (16 * w + col) * 2) = (u16)((g4 & 2) ? bf1(qv - bflo(hi)) : hi);
      LBAR();
      f32x4 a = {0.f, 0.f, 0.f, 0.f};
#pragma unroll
      for (int ks = 0; ks < 4; ++ks) a = mfma16(lds128(L + QA + (col & 3) * 256 + ks * 64 + g4 * 16), uk[ks], a);
      const float v = ((g4 & 1) ? (a[1] + a[3]) : (a[0] + a[2])) * (0.08838834764831845f * 1.4426950408889634f);
      const unsigned bits = bf1(v);
      const unsigned n1 = dppu<0x55>(bits), n2 = dppu<0xAA>(bits), n3 = dppu<0xFF>(bits);
      if ((lane & 3) == 0 && g4 < 2) {
        const v4u gr = {tagA, bits | (n1 << 16), tagB, n2 | (n3 << 16)};
        STG(gr, WS_QABS + ((g4 * 32 + h) * 128 + 32 * j + 4 * w + (col >> 2)) * 16);
      }
    }
    STAMP(4);
    const int pidx = h * 4 + j;
    const bool typeA = kk < 9;
    const int ia = xcd * 9 + kk;
    const int r2 = typeA ? 4032 + 8 * ia : 72 * (xcd * 7 + kk - 9);
    {
      LBAR();
      if (T_HEADB > 0) wait_until(t0 + T_HEADB);
      {
        const u16* src = wuv + (size_t)(128 * j) * 4096 + h * ND + tile_lane_off(lane, 4096) + (16 * (w & 1) + 4 * (w >> 2)) * 4096 + 64 * ((w >> 1) & 1);
#pragma unroll
        for (int i = 0; i < 4; ++i) DMA16(src + (size_t)i * 32 * 4096, WUV + (i * 8 + w) * 1024);
        const u16* so = wo + (size_t)(h * ND + 32 * j) * NM + tile_lane_off(lane, NM) + (16 * (w & 1) + 4 * ((w >> 1) & 1)) * NM + 64 * (w >> 2);
#pragma unroll
        for (int i = 0; i < 5; ++i)
          if (i < 4 || w < 4) DMA16(so + 128 * i, WO + (i * 8 + w) * 1024);
      }
      {
        const int r8 = lane >> 3;
        const u16* s3 = w2 + (size_t)(r2 + (typeA ? 0 : 64) + r8) * NM + 8 * ((lane & 7) ^ ((r8 >> 1) << 1));
        DMA16(s3 + 64 * w, W2X + w * 1024);
        if (w == 0) DMA16(s3 + 512, W2X + 8192);
      }
      if (!typeA) {
        const u16* s2 = w2 + (size_t)r2 * NM + tile_lane_off(lane, NM) + (32 * (w >> 2) + 16 * (w & 1) + 4 * ((w >> 1) & 1)) * NM;
#pragma unroll
        for (int i = 0; i < 9; ++i) DMA16(s2 + 64 * i, W2 + (i * 8 + w) * 1024);
      } else {
        const int f0 = 64 * ia;
        v4u xm[9];
        unsigned b1v;
        const u16* xp = x + (col & 1) * NM + 288 * (w >> 2) + 8 * g4;
        asm volatile("global_load_dwordx4 %0, %10, off\n\tglobal_load_dwordx4 %1, %10, off offset:64\n\tglobal_load_dwordx4 %2, %10, off offset:128\n\t"
                     "global_load_dwordx4 %3, %10, off offset:192\n\tglobal_load_dwordx4 %4, %10, off offset:256\n\tglobal_load_dwordx4 %5, %10, off offset:320\n\t"
                     "global_load_dwordx4 %6, %10, off offset:384\n\tglobal_load_dwordx4 %7, %10, off offset:448\n\tglobal_load_dwordx4 %8, %10, off offset:512\n\t"
                     "global_load_ushort %9, %11, off"
                     : "=&v"(xm[0]), "=&v"(xm[1]), "=&v"(xm[2]), "=&v"(xm[3]), "=&v"(xm[4]), "=&v"(xm[5]), "=&v"(xm[6]), "=&v"(xm[7]), "=&v"(xm[8]), "=&v"(b1v)
                     : "v"(xp), "v"(b1 + f0 + lane) : "memory");
        const u16* s1 = w1 + f0 + tile_lane_off(lane, NF) + (size_t)(32 * (w >> 2) + 16 * (w & 1) + 4 * ((w >> 1) & 1)) * NF;
#pragma unroll
        for (int i = 0; i < 9; ++i) DMA16(s1 + (size_t)i * 64 * NF, W1 + (i * 8 + w) * 1024);
        asm volatile("s_waitcnt vmcnt(0)\n\ts_barrier" : "+v"(xm[0]), "+v"(xm[1]), "+v"(xm[2]), "+v"(xm[3]), "+v"(xm[4]), "+v"(xm[5]), "+v"(xm[6]), "+v"(xm[7]), "+v"(xm[8]), "+v"(b1v) :: "memory");
        STAMP(10);
        {
          LAS char* p = L + W1 + (w >> 2) * 9 * 4096 + tile_tr_off(lane, w & 3);
          f32x4 acc = {0.f, 0.f, 0.f, 0.f};
#pragma unroll
          for (int i = 0; i < 9; ++i) acc = mfma16(xm[i], trfrag(p + i * 4096, p + i * 4096 + 2048), acc);
          if (g4 == 0) *(LAS f32x2*)(L + HR + (w >> 2) * 512 + (16 * (w & 3) + col) * 8) = f32x2{acc[0], acc[1]};
        }
        LBAR();
        if (w == 0) {
          const f32x2 v = *(LAS f32x2*)(L + HR + lane * 8) + *(LAS f32x2*)(L + HR + 512 + lane * 8);
          const float r0 = fmaxf(v.x + bflo(b1v), 0.f), r1 = fmaxf(v.y + bflo(b1v), 0.f);
          const v4u gr = {tagA, __float_as_uint(r0 * r0), tagB, __float_as_uint(r1 * r1)};
          STG(gr, WS_HX + (f0 + lane) * 16);
        }
      }
      STAMP(11);
    }
    asm volatile("s_waitcnt vmcnt(0) lgkmcnt(0)\n\ts_barrier" ::: "memory");
    float g0, g1;
    v4u uv[4], wf[5];
    {
      LAS char* p = L + WUV + (w >> 2) * 2048 + tile_tr_off(lane, w & 3);
#pragma unroll
      for (int i = 0; i < 4; ++i) uv[i] = trfrag(p + i * 8192, p + i * 8192 + 4096);
      LAS char* po = L + WO + (w >> 2) * 4096 + tile_tr_off(lane, w & 3);
#pragma unroll
      for (int i = 0; i < 5; ++i) wf[i] = (i < 4 || w < 4) ? trfrag(po + i * 8192, po + i * 8192 + 2048) : v4u{0u, 0u, 0u, 0u};
    }
    {
      const int gc = lane + 64 < 72 ? lane + 64 : 0;
      const v4u gxa = gld(x + lane * 8), gxb = gld(x + NM + lane * 8), ggw = gld(gw + lane * 8);
      const v4u gya = gld(x + gc * 8), gyb = gld(x + NM + gc * 8), ggy = gld(gw + gc * 8);
      {
        float d0 = 0.f, d1 = 0.f, e0 = 0.f, e1 = 0.f;
#pragma unroll
        for (int e = 0; e < 4; ++e) {
          d0 += bflo(gxa[e]) * bflo(ggw[e]) + bfhi(gxa[e]) * bfhi(ggw[e]);
          d1 += bflo(gxb[e]) * bflo(ggw[e]) + bfhi(gxb[e]) * bfhi(ggw[e]);
          e0 += bflo(gya[e]) * bflo(ggy[e]) + bfhi(gya[e]) * bfhi(ggy[e]);
          e1 += bflo(gyb[e]) * bflo(ggy[e]) + bfhi(gyb[e]) * bfhi(ggy[e]);
        }
        if (lane < 8) { d0 += e0; d1 += e1; }
        d0 = wave_sum(d0); d1 = wave_sum(d1);
        g0 = 1.f / (1.f + __expf(-d0)); g1 = 1.f / (1.f + __expf(-d1));
      }
    }
    STAMP(5);
    const int b = w >> 2, q4 = w & 3;
    float wgt;
    {
      const int off = WS_STAT + (((h * 8 + 2 * j) * 2 + b) * 64 + lane) * 16;
      v4u g;
      if (q4 == 0) {
        wait_until(t0 + T_STATS);
        for (unsigned spins = 0;; ++spins) {
          CBAR();
          g = LDG(off);
          const v4u g2 = LDG(off + 2048);
          if (__all(TAGOK(g) & TAGOK(g2)) || spins > SPIN_CAP) break;
          __builtin_amdgcn_s_sleep(POLL_SLEEP);
        }
      }
      asm volatile("s_barrier" ::: "memory");
      if (q4 != 0) g = LDG(off);
      __builtin_amdgcn_fence(__ATOMIC_ACQUIRE, "wavefront");
      v4u o[4];
      const int ooff = WS_OPART + ((((h * 4 + j) * 2 + b) * 64 + 16 * q4) * 256) + lane * 16;
#pragma unroll
      for (int i = 0; i < 4; ++i) o[i] = LDG(ooff + i * 1024);
      SCHED();
      STAMPW(0, 6);
      const float ms = __uint_as_float(g.y), ls = __uint_as_float(g.w);
      const float Mx = wave_max(ms);
      const float e = __builtin_amdgcn_exp2f(ms - Mx);
      const float Ls = wave_sum(e * ls);
      wgt = e * __builtin_amdgcn_rcpf(Ls);
      float ww[4];
#pragma unroll
      for (int i = 0; i < 4; ++i) ww[i] = __uint_as_float((unsigned)__builtin_amdgcn_ds_bpermute((16 * q4 + 4 * i + g4) * 4, (int)__float_as_uint(wgt)));
      float acc[8] = {0.f, 0.f, 0.f, 0.f, 0.f, 0.f, 0.f, 0.f};
#pragma unroll
      for (int i = 0; i < 4; ++i) {
        acc[0] += ww[i] * bflo(o[i].x); acc[1] += ww[i] * bfhi(o[i].x);
        acc[2] += ww[i] * bflo(o[i].y); acc[3] += ww[i] * bfhi(o[i].y);
        acc[4] += ww[i] * bflo(o[i].z); acc[5] += ww[i] * bfhi(o[i].z);
        acc[6] += ww[i] * bflo(o[i].w); acc[7] += ww[i] * bfhi(o[i].w);
      }
      float r4[4];
#pragma unroll
      for (int e = 0; e < 4; ++e) {
        auto t = __builtin_amdgcn_permlane32_swap(__float_as_uint(acc[e]), __float_as_uint(acc[4 + e]), false, false);
        r4[e] = sum16(__uint_as_float(t[0]) + __uint_as_float(t[1]));
      }
      if ((g4 & 1) == 0) *(LAS f32x4*)(L + RED + w * 512 + col * 32 + (lane >> 5) * 16) = f32x4{r4[0], r4[1], r4[2], r4[3]};
    }
    LBAR();
    if (tid < 128) {
      LAS float* r = (LAS float*)(L + RED) + tid;
      put_hilo(L + CTXA, 256, tid, (r[0] + r[128]) + (r[256] + r[384]), (r[512] + r[640]) + (r[768] + r[896]));
    }
    LBAR();
    {
      f32x4 acc = {0.f, 0.f, 0.f, 0.f};
#pragma unroll
      for (int i = 0; i < 4; ++i) acc = mfma16(lds128(L + CTXA + (col & 3) * 256 + i * 64 + g4 * 16), uv[i], acc);
      if (g4 == 0) {
        const v4u gr = {tagA, __float_as_uint(acc[0] + acc[2]), tagB, __float_as_uint(acc[1] + acc[3])};
        STG(gr, WS_CX + (((h * 4 + (w >> 1)) * 4 + j) * 32 + 16 * (w & 1) + col) * 16);
      }
    }
    STAMP(7);
    f32x2 mpa = {0.f, 0.f}, mpb = {0.f, 0.f};
    {
      v4u hg, hx;
      const int hoff = WS_HX + (r2 + (typeA ? (lane & 7) : lane)) * 16, xoff = WS_HX + (r2 + (typeA ? 0 : 64) + (lane & 7)) * 16;
      for (unsigned spins = 0;; ++spins) {
        CBAR();
        hg = LDG(hoff); hx = LDG(xoff);
        if (__all(TAGOK(hg) & TAGOK(hx)) || spins > SPIN_CAP) break;
        __builtin_amdgcn_s_sleep(POLL_SLEEP);
      }
      LAS char* ha = L + RED + w * 512;
      if (lane < 32) put_hilo(pv, 64, lane, lane < 8 ? __uint_as_float(hx.y) : 0.f, lane < 8 ? __uint_as_float(hx.w) : 0.f);
      const v4u hax = lds128(pv + (col & 3) * 64 + g4 * 16);
      f32x4 acc[5];
      {
        const int q = (lane >> 2) & 3;
        LAS char* p = L + W2X + (w >> 2) * 1024 + q * 128 + (lane & 3) * 8;
        const int o0 = (((w & 3) ^ (q >> 1)) * 32), o1 = 512 + (((w & 3) ^ (2 + (q >> 1))) * 32);
#pragma unroll
        for (int i = 0; i < 5; ++i) {
          acc[i] = f32x4{0.f, 0.f, 0.f, 0.f};
          if (i < 4 || w < 4) acc[i] = mfma16(hax, trfrag(p + i * 2048 + o0, p + i * 2048 + o1), acc[i]);
        }
      }
      if (!typeA) {
        put_hilo(ha, 128, lane, __uint_as_float(hg.y), __uint_as_float(hg.w));
        const v4u ha0 = lds128(ha + (col & 3) * 128 + g4 * 16), ha1 = lds128(ha + (col & 3) * 128 + 64 + g4 * 16);
        LAS char* p = L + W2 + (w >> 2) * 8192 + tile_tr_off(lane, w & 3);
#pragma unroll
        for (int i = 0; i < 5; ++i) {
          if (i < 4 || w < 4) {
            acc[i] = mfma16(ha0, trfrag(p + i * 16384, p + i * 16384 + 2048), acc[i]);
            acc[i] = mfma16(ha1, trfrag(p + i * 16384 + 4096, p + i * 16384 + 6144), acc[i]);
          }
        }
      }
      const f32x4 a = g4 == 0 ? acc[0] : g4 == 1 ? acc[1] : g4 == 2 ? acc[2] : acc[3];
      mpa = f32x2{(1.f - g0) * (a[0] + a[2]), (1.f - g1) * (a[1] + a[3])};
      mpb = f32x2{(1.f - g0) * (acc[4][0] + acc[4][2]), (1.f - g1) * (acc[4][1] + acc[4][3])};
    }
    {
      v4u ga, gb;
      const int off = WS_CX + ((h * 4 + j) * 128 + lane) * 16;
      for (unsigned spins = 0;; ++spins) {
        CBAR();
        ga = LDG(off); gb = LDG(off + 1024);
        if (__all(TAGOK(ga) & TAGOK(gb)) || spins > SPIN_CAP) break;
        __builtin_amdgcn_s_sleep(1);
      }
      STAMP(8);
      const float v0 = sum32(__uint_as_float(ga.y) + __uint_as_float(gb.y)), v1 = sum32(__uint_as_float(ga.w) + __uint_as_float(gb.w));
      if (lane < 32) put_hilo(pv, 64, lane, v0, v1);
      const v4u ca = lds128(pv + (col & 3) * 64 + g4 * 16);
      f32x4 acc[5];
#pragma unroll
      for (int i = 0; i < 5; ++i) acc[i] = mfma16(ca, wf[i], f32x4{0.f, 0.f, 0.f, 0.f});
      const f32x4 a = g4 == 0 ? acc[0] : g4 == 1 ? acc[1] : g4 == 2 ? acc[2] : acc[3];
      {
        const int ct = w + 8 * g4;
        const f32x2 mp = mpa;
        const v4u gr = {tagA, __float_as_uint(g0 * (a[0] + a[2]) + mp.x), tagB, __float_as_uint(g1 * (a[1] + a[3]) + mp.y)};
        STG(gr, WS_POUT + ((2 * ct + (col >> 3)) * 128 + pidx) * 128 + (col & 7) * 16);
      }
      if (w < 4 && g4 == 0) {
        const int ct = w + 32;
        const f32x2 mp = mpb;
        const v4u gr = {tagA, __float_as_uint(g0 * (acc[4][0] + acc[4][2]) + mp.x), tagB, __float_as_uint(g1 * (acc[4][1] + acc[4][3]) + mp.y)};
        STG(gr, WS_POUT + ((2 * ct + (col >> 3)) * 128 + pidx) * 128 + (col & 7) * 16);
      }
    }
    STAMP(9);
    STAMP_FLUSH();
    __builtin_amdgcn_endpgm();
  } else {
    const int a = xcd * 16 + (kk - 16), b = a >> 6, s = a & 63;
    constexpr int QO = 131072, XCH = QO, PB = QO + 16384, MX = QO + 24576, LS = QO + 25600;
    u64 epoch;
    asm volatile("global_load_dwordx2 %0, %1, off sc1" : "=&v"(epoch) : "v"(ws + WS_EPOCH) : "memory");
    {
      const u16* src = ckv + ((size_t)b * NSK + 128 * s + 16 * w) * NC;
#pragma unroll
      for (int i = 0; i < 16; ++i) {
        if (i == CKV_N1) wait_until(t0 + T_CKV);
        DMA16(src + i * NC + ((lane ^ fsw(i)) * 8), (16 * w + i) * 1024);
        if (i < CKV_N1 && i >= CKV_WIN - 1) asm volatile("s_waitcnt vmcnt(%0)" :: "i"(CKV_WIN - 1) : "memory");
        if (i >= CKV_N1 && i < 15 && CKV_WIN2 < 16) asm volatile("s_waitcnt vmcnt(%0)" :: "i"(CKV_WIN2 - 1) : "memory");
      }
    }
    const int r32 = lane & 31, hf = lane >> 5, kt = w & 3, ch = w >> 2;
    const unsigned fr = fsw(r32 & 15);
    const unsigned ob = r32 * 1024 + ch * 512 + ((hf ^ fr) << 4);
    asm volatile("s_waitcnt vmcnt(0)\n\ts_barrier" : "+v"(epoch) :: "memory");
    epoch += 1ull;
    const unsigned tagA = (unsigned)epoch, tagB = (unsigned)(epoch >> 32);
    STAMP(1);
    v4u kf[16], vf[8][2];
    {
      const int gl = (w & 1) * 64 + lane, hw = w >> 1;
      const int off = WS_QABS + ((b * 32 + hw) * 128 + gl) * 16;
      v4u g[8];
#pragma unroll
      for (int u = 0; u < 8; ++u) g[u] = LDG(off + u * 8192);
      SCHED();
#pragma unroll
      for (int st = 0; st < 16; ++st) kf[st] = lds128(L + kt * 32768 + ((ob ^ ((st & 7) << 5)) + (st >> 3) * 256));
      const int blk = (lane >> 4) & 1, q = (lane >> 2) & 3, p = lane & 3;
      unsigned va[2][2];
#pragma unroll
      for (int t = 0; t < 2; ++t)
#pragma unroll
        for (int cc = 0; cc < 2; ++cc)
          va[t][cc] = (8 * hf + 4 * t + q) * 1024 + (((8 * w + 4 * cc + 2 * blk + (p >> 1)) ^ (4 * q + 2 * (hf ^ t) + hf)) << 4) + 8 * (p & 1);
#pragma unroll
      for (int ks = 0; ks < 8; ++ks)
#pragma unroll
        for (int cc = 0; cc < 2; ++cc) vf[ks][cc] = trfrag(L + va[0][cc] + ks * 16384, L + va[1][cc] + ks * 16384);
      SCHED();
      unsigned todo = 0;
#pragma unroll
      for (int u = 0; u < 8; ++u) if (!__all(TAGOK(g[u]))) todo |= 1u << u;
      for (unsigned spins = 0; todo != 0 && spins < SPIN_CAP; ++spins) {
        __builtin_amdgcn_s_sleep(POLL_SLEEP);
        CBAR();
#pragma unroll
        for (int u = 0; u < 8; ++u) if (todo & (1u << u)) g[u] = LDG(off + u * 8192);
#pragma unroll
        for (int u = 0; u < 8; ++u) if (todo & (1u << u)) { if (__all(TAGOK(g[u]))) todo &= ~(1u << u); }
      }
      const unsigned qb = QO + hw * 1024 + (((gl >> 1) ^ (hw << 2)) << 4) + (gl & 1) * 8;
#pragma unroll
      for (int u = 0; u < 8; ++u) *(LAS v2u*)(L + ((qb ^ (fsw((4 * u) & 15) << 4)) + u * 4096)) = v2u{g[u].y, g[u].w};
      STAMP(2);
    }
    LBAR();
    STAMP(3);
    CYC(0);
    f32x16 S;
#pragma unroll
    for (int i = 0; i < 16; ++i) S[i] = 0.f;
    {
      v4u qf[16];
#pragma unroll
      for (int st = 0; st < 16; ++st) qf[st] = lds128(L + QO + ((ob ^ ((st & 7) << 5)) + (st >> 3) * 256));
      SCHED();
#pragma unroll
      for (int st = 0; st < 16; ++st) S = mfma32(kf[st], qf[st], S);
    }
    CYC(1);
    LBAR();
    CYC(2);
    float sv[8];
    if (ch == 0) {
      LAS f32x4* xw = (LAS f32x4*)(L + XCH + w * 2048 + lane * 16);
      xw[0] = f32x4{S[8], S[9], S[10], S[11]}; xw[64] = f32x4{S[12], S[13], S[14], S[15]};
#pragma unroll
      for (int e = 0; e < 8; ++e) sv[e] = S[e];
    } else {
      LAS f32x4* xw = (LAS f32x4*)(L + XCH + w * 2048 + lane * 16);
      xw[0] = f32x4{S[0], S[1], S[2], S[3]}; xw[64] = f32x4{S[4], S[5], S[6], S[7]};
#pragma unroll
      for (int e = 0; e < 8; ++e) sv[e] = S[8 + e];
    }
    LBAR();
    CYC(3);
    {
      LAS f32x4* xr = (LAS f32x4*)(L + XCH + (w ^ 4) * 2048 + lane * 16);
      const f32x4 p0 = xr[0], p1 = xr[64];
#pragma unroll
      for (int e = 0; e < 4; ++e) { sv[e] += p0[e]; sv[4 + e] += p1[e]; }
      float m = fmaxf(fmaxf(fmaxf(sv[0], sv[1]), fmaxf(sv[2], sv[3])), fmaxf(fmaxf(sv[4], sv[5]), fmaxf(sv[6], sv[7])));
      m = max32(m);
      *(LAS float*)(L + MX + r32 * 32 + w * 4) = m;
    }
    LBAR();
    CYC(4);
    float Mx;
    {
      const f32x4 m0 = *(LAS f32x4*)(L + MX + r32 * 32), m1 = *(LAS f32x4*)(L + MX + r32 * 32 + 16);
      Mx = fmaxf(fmaxf(fmaxf(m0[0], m0[1]), fmaxf(m0[2], m0[3])), fmaxf(fmaxf(m1[0], m1[1]), fmaxf(m1[2], m1[3])));
      float l = 0.f;
#pragma unroll
      for (int e = 0; e < 8; ++e) { sv[e] = __builtin_amdgcn_exp2f(sv[e] - Mx); l += sv[e]; }
#pragma unroll
      for (int i = 0; i < 2; ++i)
        *(LAS v2u*)(L + PB + r32 * 256 + (((4 * kt + 2 * ch + i) ^ fr) << 4) + hf * 8) = v2u{pk2(sv[4 * i], sv[4 * i + 1]), pk2(sv[4 * i + 2], sv[4 * i + 3])};
      l = sum32(l);
      *(LAS float*)(L + LS + r32 * 32 + w * 4) = l;
    }
    LBAR();
    CYC(5);
    STAMP(4);
    float Ls;
    f32x16 O0, O1;
#pragma unroll
    for (int i = 0; i < 16; ++i) { O0[i] = 0.f; O1[i] = 0.f; }
    {
      const unsigned pb = PB + r32 * 256 + ((hf ^ fr) << 4);
      v4u pf[8];
#pragma unroll
      for (int ks = 0; ks < 8; ++ks) pf[ks] = lds128(L + (pb ^ (ks << 5)));
      const f32x4 l0 = *(LAS f32x4*)(L + LS + r32 * 32), l1 = *(LAS f32x4*)(L + LS + r32 * 32 + 16);
      SCHED();
#pragma unroll
      for (int ks = 0; ks < 8; ++ks) {
        O0 = mfma32(vf[ks][0], pf[ks], O0);
        O1 = mfma32(vf[ks][1], pf[ks], O1);
      }
      Ls = ((l0[0] + l0[1]) + (l0[2] + l0[3])) + ((l1[0] + l1[1]) + (l1[2] + l1[3]));
    }
    CYC(6);
    {
      LAS char* ot = L + w * 4096;
      const unsigned ow = r32 * 128 + 8 * hf;
#pragma unroll
      for (int gq = 0; gq < 4; ++gq) {
        *(LAS v2u*)(ot + ow + ((gq ^ (r32 & 7)) << 4)) = v2u{pk2(O0[4 * gq], O0[4 * gq + 1]), pk2(O0[4 * gq + 2], O0[4 * gq + 3])};
        *(LAS v2u*)(ot + ow + (((4 + gq) ^ (r32 & 7)) << 4)) = v2u{pk2(O1[4 * gq], O1[4 * gq + 1]), pk2(O1[4 * gq + 2], O1[4 * gq + 3])};
      }
      CYC(7);
      STAMP(5);
      const int go = WS_OPART + (((((lane >> 3) * 4 + (w >> 1)) * 2 + b) * 64 + s) * 256) + (w & 1) * 128 + (((lane & 7) ^ ((lane >> 3) & 7)) << 4);
#pragma unroll
      for (int i = 0; i < 4; ++i) STG(lds128(ot + i * 1024 + lane * 16), go + i * (8 * 4 * 2 * 64 * 256));
    }
    SCHED();
    CYC(8);
    DRAIN();
    CYC(9);
    if (lane < 32) {
      const v4u gr = {tagA, __float_as_uint(Mx), tagB, __float_as_uint(Ls)};
      STG(gr, WS_STAT + (((lane * 8 + w) * 2 + b) * 64 + s) * 16);
    }
    CYC(10);
    STAMP(6);
    if ((a & 15) < 9) {
      const int r = (a >> 4) * 9 + (a & 15), t2 = tid - 256;
      float g0 = 0.f, g1 = 0.f, bias2 = 0.f;
      if (w == 4) {
        bias2 = bflo((unsigned)b2[8 * r + (lane & 7)]);
        float d0 = 0.f, d1 = 0.f;
#pragma unroll
        for (int i = 0; i < 2; ++i) {
          const int c = lane + 64 * i;
          if (c < 72) {
            const v4u xa = gld(x + c * 8), xb = gld(x + NM + c * 8), gg = gld(gw + c * 8);
#pragma unroll
            for (int e = 0; e < 4; ++e) {
              d0 += bflo(xa[e]) * bflo(gg[e]) + bfhi(xa[e]) * bfhi(gg[e]);
              d1 += bflo(xb[e]) * bflo(gg[e]) + bfhi(xb[e]) * bfhi(gg[e]);
            }
          }
        }
        d0 = wave_sum(d0); d1 = wave_sum(d1);
        g0 = 1.f / (1.f + __expf(-d0)); g1 = 1.f / (1.f + __expf(-d1));
      }
      const int off = WS_POUT + r * 16384 + tid * 16;
      v4u ga[2];
      wait_until(t0 + T_RED);
      for (unsigned spins = 0;; ++spins) {
        CBAR();
        ga[0] = LDG(off);
        if (__all(TAGOK(ga[0])) || spins > SPIN_CAP) break;
        __builtin_amdgcn_s_sleep(POLL_SLEEP);
      }
      for (unsigned spins = 0;; ++spins) {
        CBAR();
#pragma unroll
        for (int i = 0; i < 2; ++i) ga[i] = LDG(off + i * 8192);
        if (__all(TAGOK(ga[0]) & TAGOK(ga[1])) || spins > SPIN_CAP) break;
        __builtin_amdgcn_s_sleep(POLL_SLEEP);
      }
      STAMP(9);
      float v0 = __uint_as_float(ga[0].y) + __uint_as_float(ga[1].y);
      float v1 = __uint_as_float(ga[0].w) + __uint_as_float(ga[1].w);
      v0 += dppf<0x128>(v0); v1 += dppf<0x128>(v1);
      v0 = sum32(sum16(v0)); v1 = sum32(sum16(v1));
      if (lane < 8) { *(LAS float*)(L + 65536 + (lane * 8 + w) * 4) = v0; *(LAS float*)(L + 65536 + 256 + (lane * 8 + w) * 4) = v1; }
      LBAR();
      if (t2 >= 0 && t2 < 16) {
        const f32x4 p0 = *(LAS f32x4*)(L + 65536 + t2 * 32), p1 = *(LAS f32x4*)(L + 65536 + t2 * 32 + 16);
        const float v = ((p0[0] + p0[1]) + (p0[2] + p0[3])) + ((p1[0] + p1[1]) + (p1[2] + p1[3])) + (1.f - (t2 < 8 ? g0 : g1)) * bias2;
        out[(t2 >> 3) * NM + 8 * r + (t2 & 7)] = (u16)bf1(v);
      }
      if (r == 0 && tid == 0) __hip_atomic_store((GAS u64*)(ws + WS_EPOCH), epoch, __ATOMIC_RELAXED, __HIP_MEMORY_SCOPE_AGENT);
      STAMP(10);
    }
    STAMP_FLUSH();
  }
}

extern "C" void kernel_launch(void* const* d_in, const int* in_sizes, int n_in, void* d_out, int out_size, void* d_ws, size_t ws_size,
                              hipStream_t stream) {
  fused_kernel<<<256, 512, 0, stream>>>((const u16*)d_in[0], (const u16*)d_in[1], (const u16*)d_in[2], (const u16*)d_in[3],
                                        (const u16*)d_in[4], (const u16*)d_in[5], (const u16*)d_in[7], (const u16*)d_in[8],
                                        (const u16*)d_in[9], (const u16*)d_in[10], (const u16*)d_in[11], (u16*)d_out, (char*)d_ws);
}
```

```cpp
#include <hip/hip_runtime.h>
#include <hip/hip_bf16.h>
#include <stdint.h>

#define GAS __attribute__((address_space(1)))
#define LAS __attribute__((address_space(3)))
#define DI __device__ __forceinline__
typedef unsigned short u16;
typedef unsigned long long u64;
typedef unsigned v4u __attribute__((ext_vector_type(4)));
typedef unsigned v2u __attribute__((ext_vector_type(2)));
typedef float f32x2 __attribute__((ext_vector_type(2)));
typedef float f32x4 __attribute__((ext_vector_type(4)));
typedef float f32x16 __attribute__((ext_vector_type(16)));
typedef short bf16x8 __attribute__((ext_vector_type(8)));
typedef short s16x4 __attribute__((ext_vector_type(4)));
typedef __bf16 bf2_t __attribute__((ext_vector_type(2)));

constexpr int NM = 576, NH = 32, ND = 128, NC = 512, NSK = 8192, NF = 4608;
constexpr int WS_EPOCH = 0;
constexpr int WS_HX = 4096;
constexpr int WS_QX = WS_HX + 4 * 73728;
constexpr int WS_CX = WS_QX + 262144;
constexpr int WS_QABS = WS_CX + 262144;
constexpr int WS_STAT = WS_QABS + 131072;
constexpr int WS_POUT = WS_STAT + 524288;
constexpr int WS_OPART = WS_POUT + 72 * 128 * 128;
constexpr int WS_STAMP = WS_OPART + 4194304;
constexpr int WS_BYTES = WS_STAMP + 2 * 256 * 256;
constexpr int AUX_SC1 = 16;
constexpr unsigned SPIN_CAP = 1u << 17;

#define CKV_N1 0
#define CKV_WIN 2
#define CKV_WIN2 16
#define PRETOUCH 0
#define XPOL ""
#define STAT_SW 1
#define STAT_SLEEP 2
#define OAUX 16
#define OPRE 0
#define WS_BASE 0
#define HO0_SLEEP 3
#define T_CKV 150
#define POLL_SLEEP 2
#define T_STATS 850
#define T_HEADB 470
#define T_RED 800

#define STAMP(i) do {} while (0)
#define STAMPW(wv, i) do {} while (0)
#define STAMP_FLUSH() do {} while (0)
#define CYC(i) do {} while (0)

DI unsigned fsw(unsigned r) { return ((r & 3u) << 2) | ((((r >> 3) ^ (r >> 2)) & 1u) << 1) | ((r >> 3) & 1u); }
DI float bflo(unsigned v) { return __uint_as_float(v << 16); }
DI float bfhi(unsigned v) { return __uint_as_float(v & 0xffff0000u); }
DI unsigned pk2(float a, float b) { f32x2 f = {a, b}; bf2_t r = __builtin_convertvector(f, bf2_t); return __builtin_bit_cast(unsigned, r); }
DI unsigned bf1(float a) { return pk2(a, 0.f) & 0xffffu; }
DI f32x4 mfma16(v4u a, v4u b, f32x4 c) { return __builtin_amdgcn_mfma_f32_16x16x32_bf16(__builtin_bit_cast(bf16x8, a), __builtin_bit_cast(bf16x8, b), c, 0, 0, 0); }
DI f32x16 mfma32(v4u a, v4u b, f32x16 c) { return __builtin_amdgcn_mfma_f32_32x32x16_bf16(__builtin_bit_cast(bf16x8, a), __builtin_bit_cast(bf16x8, b), c, 0, 0, 0); }
DI v2u trread(LAS char* p) { s16x4 r = __builtin_amdgcn_ds_read_tr16_b64_v4i16((LAS s16x4*)p); return __builtin_bit_cast(v2u, r); }
DI v4u trfrag(LAS char* p0, LAS char* p1) { v2u a = trread(p0), b = trread(p1); return v4u{a.x, a.y, b.x, b.y}; }
DI v4u lds128(LAS char* p) { return *(LAS v4u*)p; }
DI void dma16(const u16* g, LAS char* l) { __builtin_amdgcn_global_load_lds((const GAS unsigned*)g, (LAS unsigned*)l, 16, 0, 2); }
DI v4u gld(const u16* p) { return *(const GAS v4u*)p; }
DI v4u gldnt(const u16* p) { return __builtin_nontemporal_load((const GAS v4u*)p); }
DI v2u gldnt8(const u16* p) { return __builtin_nontemporal_load((const GAS v2u*)p); }
template <int CTRL> DI float dppf(float x) { return __builtin_bit_cast(float, __builtin_amdgcn_mov_dpp(__builtin_bit_cast(int, x), CTRL, 0xf, 0xf, true)); }
template <int CTRL> DI unsigned dppu(unsigned x) { return (unsigned)__builtin_amdgcn_mov_dpp((int)x, CTRL, 0xf, 0xf, true); }
DI float sum32(float x) { auto t = __builtin_amdgcn_permlane32_swap(__float_as_uint(x), __float_as_uint(x), false, false); return __uint_as_float(t[0]) + __uint_as_float(t[1]); }
DI float max32(float x) { auto t = __builtin_amdgcn_permlane32_swap(__float_as_uint(x), __float_as_uint(x), false, false); return fmaxf(__uint_as_float(t[0]), __uint_as_float(t[1])); }
DI float sum16(float x) { auto t = __builtin_amdgcn_permlane16_swap(__float_as_uint(x), __float_as_uint(x), false, false); return __uint_as_float(t[0]) + __uint_as_float(t[1]); }
DI float max16(float x) { auto t = __builtin_amdgcn_permlane16_swap(__float_as_uint(x), __float_as_uint(x), false, false); return fmaxf(__uint_as_float(t[0]), __uint_as_float(t[1])); }
DI float wave_sum(float x) { x += dppf<0xB1>(x); x += dppf<0x4E>(x); x += dppf<0x141>(x); x += dppf<0x128>(x); x = sum16(x); return sum32(x); }
DI float wave_max(float x) { x = fmaxf(x, dppf<0xB1>(x)); x = fmaxf(x, dppf<0x4E>(x)); x = fmaxf(x, dppf<0x141>(x)); x = fmaxf(x, dppf<0x128>(x)); x = max16(x); return max32(x); }
DI void wait_until(u64 t) { while (__builtin_amdgcn_s_memrealtime() < t) __builtin_amdgcn_s_sleep(1); }
#define DRAIN() asm volatile("s_waitcnt vmcnt(0)" ::: "memory")
#define CBAR() asm volatile("" ::: "memory")
#define SCHED() __builtin_amdgcn_sched_barrier(0)
#define LBAR() asm volatile("s_waitcnt lgkmcnt(0)\n\ts_barrier" ::: "memory")
#define DMA16(gp, ldsoff) do { unsigned keep_; asm volatile("s_mov_b32 %0, m0\n\ts_mov_b32 m0, %2\n\ts_nop 0\n\tglobal_load_lds_dwordx4 %1, off nt\n\ts_mov_b32 m0, %0" : "=&s"(keep_) : "v"(gp), "s"((unsigned)(size_t)L + (unsigned)(ldsoff)) : "memory"); } while (0)
#define TOUCH(dst, off) asm volatile("buffer_load_dword %0, %1, %2, 0 offen sc1" : "=&v"(dst) : "v"(off), "s"(rs) : "memory")
#define DMA16X(gp, ldsoff) do { unsigned keep_; asm volatile("s_mov_b32 %0, m0\n\ts_mov_b32 m0, %2\n\ts_nop 0\n\tglobal_load_lds_dwordx4 %1, off" XPOL "\n\ts_mov_b32 m0, %0" : "=&s"(keep_) : "v"(gp), "s"((unsigned)(size_t)L + (unsigned)(ldsoff)) : "memory"); } while (0)
#define LDG(off) __builtin_amdgcn_raw_buffer_load_b128(rs, (off), 0, AUX_SC1)
#define STG(v, off) __builtin_amdgcn_raw_buffer_store_b128((v), rs, (off), 0, AUX_SC1)
#define TAGOK(g) (((g).x == tagA) & ((g).z == tagB))

DI int tile_lane_off(int lane, int ld) { const int r8 = lane >> 3; return (8 * (r8 >> 2) + (r8 & 3)) * ld + 8 * ((lane & 7) ^ ((r8 >> 1) << 1)); }
DI int tile_tr_off(int lane, int ct) { const int r8 = 4 * ((lane >> 4) & 1) + ((lane >> 2) & 3); return (lane >> 5) * 1024 + r8 * 128 + ((ct ^ (r8 >> 1)) * 32) + (lane & 3) * 8; }
DI void put_hilo(LAS char* base, int stride, int d, float v0, float v1) {
  const unsigned h = pk2(v0, v1);
  const unsigned l = pk2(v0 - bflo(h), v1 - bfhi(h));
  *(LAS u16*)(base + d * 2) = (u16)h;
  *(LAS u16*)(base + stride + d * 2) = (u16)(h >> 16);
  *(LAS u16*)(base + 2 * stride + d * 2) = (u16)l;
  *(LAS u16*)(base + 3 * stride + d * 2) = (u16)(l >> 16);
}

__global__ void __launch_bounds__(512, 2)
fused_kernel(const u16* __restrict__ x, const u16* __restrict__ ckv, const u16* __restrict__ wq, const u16* __restrict__ wuk,
             const u16* __restrict__ wuv, const u16* __restrict__ wo, const u16* __restrict__ w1, const u16* __restrict__ b1,
             const u16* __restrict__ w2, const u16* __restrict__ b2, const u16* __restrict__ gw, u16* __restrict__ out, char* ws) {
  __shared__ __attribute__((aligned(16))) char smem[163840];
  LAS char* L = (LAS char*)smem;
  const int tid = threadIdx.x, lane = tid & 63, w = __builtin_amdgcn_readfirstlane(tid >> 6);
  const int bid = blockIdx.x, xcd = bid & 7, kk = bid >> 3;
  const u64 t0 = __builtin_amdgcn_s_memrealtime();
  const __amdgpu_buffer_rsrc_t rs = __builtin_amdgcn_make_buffer_rsrc((void*)ws, 0, WS_BYTES, 0x00020000);
#define LOAD_EPOCH() (__hip_atomic_load((GAS u64*)(ws + WS_EPOCH), __ATOMIC_RELAXED, __HIP_MEMORY_SCOPE_AGENT) + 1ull)
  STAMP(0);
  const int g4 = lane >> 4, col = lane & 15;

  if (kk < 16) {
    const int h = xcd * 4 + (kk >> 2), j = kk & 3;
    constexpr int WQ = 0, WUK = 40960, W1 = 69632, W2 = W1 + 36864, W2X = 143360, WUV = 0, WO = 32768;
    constexpr int RED = 152576, CTXA = RED + 4096, QA = CTXA + 1024, PRIV = QA + 1024, HR = PRIV + 2048;
    const int ks0 = j < 2 ? 5 * j : 4 * j + 2;
    u64 epoch;
    asm volatile("global_load_dwordx2 %0, %1, off sc1" : "=&v"(epoch) : "v"(ws + WS_EPOCH) : "memory");
    constexpr int XG = W2X;
    if (w < 3) { const int c = 64 * w + lane; DMA16X(x + 8 * (c < 144 ? c : 0), XG + w * 1024); }
    else if (w < 5) { const int c = 64 * (w - 3) + lane; DMA16X(gw + 8 * (c < 72 ? c : 0), XG + w * 1024); }
    {
      const u16* src = wq + (size_t)(32 * ks0) * 4096 + h * ND + tile_lane_off(lane, 4096) + (16 * (w & 1) + 4 * (w >> 2)) * 4096 + 64 * ((w >> 1) & 1);
#pragma unroll
      for (int i = 0; i < 5; ++i)
        if (i < 4 || j < 2) DMA16(src + (size_t)i * 32 * 4096, WQ + (i * 8 + w) * 1024);
#pragma unroll
      for (int i = 0; i < 4; ++i) {
        const int row = 16 * w + 4 * i + g4;
        DMA16(wuk + (size_t)(128 * j + row) * 4096 + h * ND + 8 * (col ^ fsw(4 * i + g4)), WUK + (4 * w + i) * 1024);
      }
    }
    asm volatile("s_waitcnt vmcnt(4)\n\ts_barrier" : "+v"(epoch) :: "memory");
    v4u xa[5];
#pragma unroll
    for (int i = 0; i < 5; ++i) xa[i] = lds128(L + XG + (col & 1) * 1152 + 64 * (ks0 + ((i < 4 || j < 2) ? i : 0)) + 16 * g4);
    epoch += 1ull;
    const unsigned tagA = (unsigned)epoch, tagB = (unsigned)(epoch >> 32);
    STAMP(1);
    {
      f32x4 acc = {0.f, 0.f, 0.f, 0.f};
      LAS char* p = L + WQ + (w >> 2) * 2048 + tile_tr_off(lane, w & 3);
      v4u bq[5];
#pragma unroll
      for (int i = 0; i < 4; ++i) bq[i] = trfrag(p + i * 8192, p + i * 8192 + 4096);
      if (j < 2) bq[4] = trfrag(p + 4 * 8192, p + 4 * 8192 + 4096);
#pragma unroll
      for (int i = 0; i < 4; ++i) acc = mfma16(xa[i], bq[i], acc);
      if (j < 2) acc = mfma16(xa[4], bq[4], acc);
      if (g4 == 0) { const v4u gr = {tagA, __float_as_uint(acc[0]), tagB, __float_as_uint(acc[1])}; STG(gr, WS_QX + ((h * 4 + j) * 128 + 16 * w + col) * 16); }
    }
    STAMP(2);
    float g0, g1;
    {
      const int gc = lane + 64 < 72 ? lane + 64 : 0;
      const v4u gq[6] = {lds128(L + XG + lane * 16), lds128(L + XG + 1152 + lane * 16), lds128(L + XG + 3072 + lane * 16),
                         lds128(L + XG + gc * 16), lds128(L + XG + 1152 + gc * 16), lds128(L + XG + 3072 + gc * 16)};
      float d0 = 0.f, d1 = 0.f, e0 = 0.f, e1 = 0.f;
#pragma unroll
      for (int e = 0; e < 4; ++e) {
        d0 += bflo(gq[0][e]) * bflo(gq[2][e]) + bfhi(gq[0][e]) * bfhi(gq[2][e]);
        d1 += bflo(gq[1][e]) * bflo(gq[2][e]) + bfhi(gq[1][e]) * bfhi(gq[2][e]);
        e0 += bflo(gq[3][e]) * bflo(gq[5][e]) + bfhi(gq[3][e]) * bfhi(gq[5][e]);
        e1 += bflo(gq[4][e]) * bflo(gq[5][e]) + bfhi(gq[4][e]) * bfhi(gq[5][e]);
      }
      if (lane < 8) { d0 += e0; d1 += e1; }
      d0 = wave_sum(d0); d1 = wave_sum(d1);
      g0 = 1.f / (1.f + __expf(-d0)); g1 = 1.f / (1.f + __expf(-d1));
      asm volatile("" : "+v"(g0), "+v"(g1));
    }
    DRAIN();
    __builtin_amdgcn_s_sleep(HO0_SLEEP);
    LAS char* pv = L + PRIV + w * 256;
    {
      v4u ga;
      const int off = WS_QX + ((h * 4 + g4) * 128 + 16 * w + col) * 16;
      for (unsigned spins = 0;; ++spins) {
        CBAR();
        ga = LDG(off);
        if (__all(TAGOK(ga)) || spins > SPIN_CAP) break;
        __builtin_amdgcn_s_sleep(1);
      }
      STAMP(3);
      v4u uk[4];
#pragma unroll
      for (int ks = 0; ks < 4; ++ks) uk[ks] = lds128(L + WUK + (16 * w + col) * 256 + (((4 * ks + g4) ^ fsw(col)) << 4));
      const float q0 = sum32(sum16(__uint_as_float(ga.y))), q1 = sum32(sum16(__uint_as_float(ga.w)));
      const float qv = (g4 & 1) ? q1 : q0;
      const unsigned hi = bf1(qv);
      *(LAS u16*)(L + QA + g4 * 256 + (16 * w + col) * 2) = (u16)((g4 & 2) ? bf1(qv - bflo(hi)) : hi);
      LBAR();
      f32x4 a = {0.f, 0.f, 0.f, 0.f};
#pragma unroll
      for (int ks = 0; ks < 4; ++ks) a = mfma16(lds128(L + QA + (col & 3) * 256 + ks * 64 + g4 * 16), uk[ks], a);
      const float v = ((g4 & 1) ? (a[1] + a[3]) : (a[0] + a[2])) * (0.08838834764831845f * 1.4426950408889634f);
      const unsigned bits = bf1(v);
      const unsigned n1 = dppu<0x55>(bits), n2 = dppu<0xAA>(bits), n3 = dppu<0xFF>(bits);
      if ((lane & 3) == 0 && g4 < 2) {
        const v4u gr = {tagA, bits | (n1 << 16), tagB, n2 | (n3 << 16)};
        STG(gr, WS_QABS + ((g4 * 32 + h) * 128 + 32 * j + 4 * w + (col >> 2)) * 16);
      }
    }
    STAMP(4);
    const int pidx = h * 4 + j;
    const bool dbl = kk >= 14;
    const int u0 = dbl ? 2 * kk - 14 : kk;
    const int r2 = dbl ? 4480 + 8 * (xcd * 2 + kk - 14) : 40 * (xcd * 14 + kk);
    const int nk2 = dbl ? 8 : 40;
    unsigned b1v[3];
    {
      v4u xm[2][5];
      const int sub = w >> 2, kb = 5 * sub;
#pragma unroll
      for (int i = 0; i < 5; ++i) {
        const int ki = kb + ((i < 4 || sub == 0) ? i : 0);
        xm[0][i] = lds128(L + XG + (col & 1) * 1152 + 64 * (9 * (u0 & 1) + ki) + 16 * g4);
        xm[1][i] = lds128(L + XG + (col & 1) * 1152 + 64 * (9 + ki) + 16 * g4);
      }
      LBAR();
      if (T_HEADB > 0) wait_until(t0 + T_HEADB);
      {
        const u16* src = wuv + (size_t)(128 * j) * 4096 + h * ND + tile_lane_off(lane, 4096) + (16 * (w & 1) + 4 * (w >> 2)) * 4096 + 64 * ((w >> 1) & 1);
#pragma unroll
        for (int i = 0; i < 4; ++i) DMA16(src + (size_t)i * 32 * 4096, WUV + (i * 8 + w) * 1024);
        const u16* so = wo + (size_t)(h * ND + 32 * j) * NM + tile_lane_off(lane, NM) + (16 * (w & 1) + 4 * ((w >> 1) & 1)) * NM + 64 * (w >> 2);
#pragma unroll
        for (int i = 0; i < 5; ++i)
          if (i < 4 || w < 4) DMA16(so + 128 * i, WO + (i * 8 + w) * 1024);
      }
      {
        const int r8 = lane >> 3;
        const u16* s3 = w2 + (size_t)(r2 + nk2 - 8 + r8) * NM + 8 * ((lane & 7) ^ ((r8 >> 1) << 1));
        DMA16(s3 + 64 * w, W2X + w * 1024);
        if (w == 0) DMA16(s3 + 512, W2X + 8192);
      }
      if (!dbl) {
        const u16* s2 = w2 + (size_t)r2 * NM + tile_lane_off(lane, NM) + (16 * (w & 1) + 4 * ((w >> 1) & 1)) * NM + 64 * (w >> 2);
#pragma unroll
        for (int i = 0; i < 5; ++i)
          if (i < 4 || w < 4) DMA16(s2 + 128 * i, W2 + (i * 8 + w) * 1024);
      }
      {
        const int fb = r2 + col;
        asm volatile("global_load_ushort %0, %3, off\n\tglobal_load_ushort %1, %4, off\n\tglobal_load_ushort %2, %5, off"
                     : "=&v"(b1v[0]), "=&v"(b1v[1]), "=&v"(b1v[2])
                     : "v"(b1 + (col < nk2 ? fb : r2)), "v"(b1 + (16 + col < nk2 ? fb + 16 : r2)), "v"(b1 + (32 + col < nk2 ? fb + 32 : r2)) : "memory");
      }
      const int f0 = 64 * (xcd * 9 + (u0 >> 1));
      {
        const u16* s1 = w1 + f0 + tile_lane_off(lane, NF) + (size_t)(288 * (u0 & 1) + 32 * (w >> 2) + 16 * (w & 1) + 4 * ((w >> 1) & 1)) * NF;
#pragma unroll
        for (int i = 0; i < 5; ++i)
          if (i < 4 || w < 4) DMA16(s1 + (size_t)i * 64 * NF, W1 + (i * 8 + w) * 1024);
        if (dbl) {
#pragma unroll
          for (int i = 0; i < 5; ++i)
            if (i < 4 || w < 4) DMA16(s1 + (size_t)(288 + i * 64) * NF, W2 + (i * 8 + w) * 1024);
        }
      }
      asm volatile("s_waitcnt vmcnt(0)\n\ts_barrier" : "+v"(b1v[0]), "+v"(b1v[1]), "+v"(b1v[2]) :: "memory");
      STAMP(10);
#pragma unroll
      for (int un = 0; un < 2; ++un) {
        if (un == 0 || dbl) {
          LAS char* p = L + W1 + un * 36864 + kb * 4096 + tile_tr_off(lane, w & 3);
          f32x4 acc = {0.f, 0.f, 0.f, 0.f};
#pragma unroll
          for (int i = 0; i < 4; ++i) acc = mfma16(xm[un][i], trfrag(p + i * 4096, p + i * 4096 + 2048), acc);
          if (sub == 0) acc = mfma16(xm[un][4], trfrag(p + 4 * 4096, p + 4 * 4096 + 2048), acc);
          if (g4 == 0) {
            const v4u gr = {tagA, __float_as_uint(acc[0]), tagB, __float_as_uint(acc[1])};
            STG(gr, WS_HX + ((((u0 & 1) + un) * 2 + sub) * NF + f0 + 16 * (w & 3) + col) * 16);
          }
        }
      }
      STAMP(11);
    }
    v4u uv[4], wf[5];
    {
      LAS char* p = L + WUV + (w >> 2) * 2048 + tile_tr_off(lane, w & 3);
#pragma unroll
      for (int i = 0; i < 4; ++i) uv[i] = trfrag(p + i * 8192, p + i * 8192 + 4096);
      LAS char* po = L + WO + (w >> 2) * 4096 + tile_tr_off(lane, w & 3);
#pragma unroll
      for (int i = 0; i < 5; ++i) wf[i] = (i < 4 || w < 4) ? trfrag(po + i * 8192, po + i * 8192 + 2048) : v4u{0u, 0u, 0u, 0u};
    }
    v4u w2m[5], w2x[5];
    {
      const int q = (lane >> 2) & 3;
      LAS char* p = L + W2X + (w >> 2) * 1024 + q * 128 + (lane & 3) * 8;
      const int o0 = (((w & 3) ^ (q >> 1)) * 32), o1 = 512 + (((w & 3) ^ (2 + (q >> 1))) * 32);
      LAS char* pm = L + W2 + (w >> 2) * 4096 + tile_tr_off(lane, w & 3);
#pragma unroll
      for (int i = 0; i < 5; ++i) {
        const int ii = (i < 4 || w < 4) ? i : 0;
        w2x[i] = trfrag(p + ii * 2048 + o0, p + ii * 2048 + o1);
        w2m[i] = trfrag(pm + ii * 8192, pm + ii * 8192 + 2048);
      }
    }
    STAMP(5);
    const int b = w >> 2, q4 = w & 3;
    float wgt;
    v4u hg = {0u, 0u, 0u, 0u}, cxw = {0u, 0u, 0u, 0u};
    const int ht = w - 2, hf_ = 16 * ht + col;
    const int ho = WS_HX + (g4 * NF + r2 + (hf_ < nk2 ? hf_ : 0)) * 16;
    constexpr int HA = QA;
    {
      const int off = WS_STAT + (((b * 64 + 16 * q4 + ((lane >> 1) & 15)) * 8 + 2 * j + (lane & 1)) * 32 + h) * 16;
      const int ooff = WS_OPART + ((((h * 4 + j) * 2 + b) * 64 + 16 * q4) * 256) + lane * 16;
      v4u g, o[4];
      bool ok = lane >= 32;
      unsigned done = 0;
      if (w < 2) { cxw = LDG(WS_CX + ((h * 4 + j) * 128 + w * 64 + lane) * 16); asm volatile("" :: "v"(cxw)); }
      wait_until(t0 + T_STATS);
      for (unsigned spins = 0;; ++spins) {
        CBAR();
        if (!ok) { g = LDG(off); ok = TAGOK(g); }
        const unsigned m = (unsigned)__builtin_amdgcn_ballot_w64(ok) | (spins > SPIN_CAP ? 0xffffffffu : 0u);
        __builtin_amdgcn_fence(__ATOMIC_ACQUIRE, "wavefront");
#pragma unroll
        for (int i = 0; i < 4; ++i)
          if (!(done & (1u << i)) && ((m >> (8 * i)) & 0xffu) == 0xffu) { o[i] = LDG(ooff + i * 1024); done |= 1u << i; }
        if (done == 15u) break;
        __builtin_amdgcn_s_sleep(STAT_SLEEP);
      }
      if (ht >= 0 && ht < 3) hg = LDG(ho);
      if (lane < 32 && !(lane & 1)) *(LAS v2u*)(L + HR + b * 512 + (16 * q4 + (lane >> 1)) * 8) = v2u{g.y, g.w};
      LBAR();
      STAMPW(0, 6);
      { const v2u t = *(LAS v2u*)(L + HR + b * 512 + lane * 8); g.y = t.x; g.w = t.y; }
      const float ms = __uint_as_float(g.y), ls = __uint_as_float(g.w);
      const float Mx = wave_max(ms);
      const float e = __builtin_amdgcn_exp2f(ms - Mx);
      const float Ls = wave_sum(e * ls);
      wgt = e * __builtin_amdgcn_rcpf(Ls);
      float ww[4];
#pragma unroll
      for (int i = 0; i < 4; ++i) ww[i] = __uint_as_float((unsigned)__builtin_amdgcn_ds_bpermute((16 * q4 + 4 * i + g4) * 4, (int)__float_as_uint(wgt)));
      float acc[8] = {0.f, 0.f, 0.f, 0.f, 0.f, 0.f, 0.f, 0.f};
#pragma unroll
      for (int i = 0; i < 4; ++i) {
        acc[0] += ww[i] * bflo(o[i].x); acc[1] += ww[i] * bfhi(o[i].x);
        acc[2] += ww[i] * bflo(o[i].y); acc[3] += ww[i] * bfhi(o[i].y);
        acc[4] += ww[i] * bflo(o[i].z); acc[5] += ww[i] * bfhi(o[i].z);
        acc[6] += ww[i] * bflo(o[i].w); acc[7] += ww[i] * bfhi(o[i].w);
      }
      float r4[4];
#pragma unroll
      for (int e = 0; e < 4; ++e) {
        auto t = __builtin_amdgcn_permlane32_swap(__float_as_uint(acc[e]), __float_as_uint(acc[4 + e]), false, false);
        r4[e] = sum16(__uint_as_float(t[0]) + __uint_as_float(t[1]));
      }
      if ((g4 & 1) == 0) *(LAS f32x4*)(L + RED + w * 512 + col * 32 + (lane >> 5) * 16) = f32x4{r4[0], r4[1], r4[2], r4[3]};
    }
    LBAR();
    if (tid < 128) {
      LAS float* r = (LAS float*)(L + RED) + tid;
      put_hilo(L + CTXA, 256, tid, (r[0] + r[128]) + (r[256] + r[384]), (r[512] + r[640]) + (r[768] + r[896]));
    } else if (w < 5) {
      for (unsigned spins = 0; !__all(TAGOK(hg)) && spins < SPIN_CAP; ++spins) {
        __builtin_amdgcn_s_sleep(POLL_SLEEP);
        CBAR();
        hg = LDG(ho);
      }
      const float bb = bflo(ht == 0 ? b1v[0] : ht == 1 ? b1v[1] : b1v[2]);
      const float p0 = fmaxf(sum32(sum16(__uint_as_float(hg.y))) + bb, 0.f), p1 = fmaxf(sum32(sum16(__uint_as_float(hg.w))) + bb, 0.f);
      const float hv = hf_ < nk2 ? ((g4 & 1) ? p1 * p1 : p0 * p0) : 0.f;
      const unsigned hi = bf1(hv);
      const int kpos = dbl ? 32 - 16 * ht + col : hf_;
      *(LAS u16*)(L + HA + g4 * 128 + kpos * 2) = (u16)((g4 & 2) ? bf1(hv - bflo(hi)) : hi);
    } else if (w == 5) {
      *(LAS u16*)(L + HA + g4 * 128 + (48 + col) * 2) = 0;
    }
    LBAR();
    {
      f32x4 acc = {0.f, 0.f, 0.f, 0.f};
#pragma unroll
      for (int i = 0; i < 4; ++i) acc = mfma16(lds128(L + CTXA + (col & 3) * 256 + i * 64 + g4 * 16), uv[i], acc);
      if (g4 == 0) {
        const v4u gr = {tagA, __float_as_uint(acc[0] + acc[2]), tagB, __float_as_uint(acc[1] + acc[3])};
        STG(gr, WS_CX + (((h * 4 + (w >> 1)) * 4 + j) * 32 + 16 * (w & 1) + col) * 16);
      }
    }
    STAMP(7);
    f32x2 mpa, mpb;
    {
      const v4u ha0 = lds128(L + HA + (col & 3) * 128 + g4 * 16), hax = lds128(L + HA + (col & 3) * 128 + 64 + g4 * 16);
      f32x4 acc[5];
#pragma unroll
      for (int i = 0; i < 5; ++i) acc[i] = mfma16(ha0, w2m[i], mfma16(hax, w2x[i], f32x4{0.f, 0.f, 0.f, 0.f}));
      const f32x4 a = g4 == 0 ? acc[0] : g4 == 1 ? acc[1] : g4 == 2 ? acc[2] : acc[3];
      mpa = f32x2{(1.f - g0) * (a[0] + a[2]), (1.f - g1) * (a[1] + a[3])};
      mpb = f32x2{(1.f - g0) * (acc[4][0] + acc[4][2]), (1.f - g1) * (acc[4][1] + acc[4][3])};
    }
    {
      v4u ga, gb;
      const int off = WS_CX + ((h * 4 + j) * 128 + lane) * 16;
      for (unsigned spins = 0;; ++spins) {
        CBAR();
        ga = LDG(off); gb = LDG(off + 1024);
        if (__all(TAGOK(ga) & TAGOK(gb)) || spins > SPIN_CAP) break;
        __builtin_amdgcn_s_sleep(1);
      }
      STAMP(8);
      const float v0 = sum32(__uint_as_float(ga.y) + __uint_as_float(gb.y)), v1 = sum32(__uint_as_float(ga.w) + __uint_as_float(gb.w));
      if (lane < 32) put_hilo(pv, 64, lane, v0, v1);
      const v4u ca = lds128(pv + (col & 3) * 64 + g4 * 16);
      f32x4 acc[5];
#pragma unroll
      for (int i = 0; i < 5; ++i) acc[i] = mfma16(ca, wf[i], f32x4{0.f, 0.f, 0.f, 0.f});
      const f32x4 a = g4 == 0 ? acc[0] : g4 == 1 ? acc[1] : g4 == 2 ? acc[2] : acc[3];
      {
        const int ct = w + 8 * g4;
        const f32x2 mp = mpa;
        const v4u gr = {tagA, __float_as_uint(g0 * (a[0] + a[2]) + mp.x), tagB, __float_as_uint(g1 * (a[1] + a[3]) + mp.y)};
        STG(gr, WS_POUT + ((2 * ct + (col >> 3)) * 128 + pidx) * 128 + (col & 7) * 16);
      }
      if (w < 4 && g4 == 0) {
        const int ct = w + 32;
        const f32x2 mp = mpb;
        const v4u gr = {tagA, __float_as_uint(g0 * (acc[4][0] + acc[4][2]) + mp.x), tagB, __float_as_uint(g1 * (acc[4][1] + acc[4][3]) + mp.y)};
        STG(gr, WS_POUT + ((2 * ct + (col >> 3)) * 128 + pidx) * 128 + (col & 7) * 16);
      }
    }
    STAMP(9);
    STAMP_FLUSH();
    __builtin_amdgcn_endpgm();
  } else {
    const int a = xcd * 16 + (kk - 16), b = a >> 6, s = a & 63;
    constexpr int QO = 131072, XCH = 32768, PB = XCH + 16384, MX = XCH + 24576, LS = XCH + 25600;
    u64 epoch;
    asm volatile("global_load_dwordx2 %0, %1, off sc1" : "=&v"(epoch) : "v"(ws + WS_EPOCH) : "memory");
    {
      const u16* src = ckv + ((size_t)b * NSK + 128 * s + 16 * w) * NC;
#pragma unroll
      for (int i = 0; i < 16; ++i) {
        if (i == CKV_N1) wait_until(t0 + T_CKV);
        DMA16(src + i * NC + ((lane ^ fsw(i)) * 8), (16 * w + i) * 1024);
        if (i < CKV_N1 && i >= CKV_WIN - 1) asm volatile("s_waitcnt vmcnt(%0)" :: "i"(CKV_WIN - 1) : "memory");
        if (i >= CKV_N1 && i < 15 && CKV_WIN2 < 16) asm volatile("s_waitcnt vmcnt(%0)" :: "i"(CKV_WIN2 - 1) : "memory");
      }
    }
    const int r32 = lane & 31, hf = lane >> 5, kt = w & 3, ch = w >> 2;
    const unsigned fr = fsw(r32 & 15);
    const unsigned ob = r32 * 1024 + ch * 512 + ((hf ^ fr) << 4);
    asm volatile("s_waitcnt vmcnt(0)\n\ts_barrier" : "+v"(epoch) :: "memory");
    epoch += 1ull;
    const unsigned tagA = (unsigned)epoch, tagB = (unsigned)(epoch >> 32);
    STAMP(1);
    v4u kf[16], vf[8][2];
    {
      const int gl = (w & 1) * 64 + lane, hw = w >> 1;
      const int off = WS_QABS + ((b * 32 + hw) * 128 + gl) * 16;
      v4u g[8];
#pragma unroll
      for (int u = 0; u < 8; ++u) g[u] = LDG(off + u * 8192);
      SCHED();
#pragma unroll
      for (int st = 0; st < 16; ++st) kf[st] = lds128(L + kt * 32768 + ((ob ^ ((st & 7) << 5)) + (st >> 3) * 256));
      const int blk = (lane >> 4) & 1, q = (lane >> 2) & 3, p = lane & 3;
      unsigned va[2][2];
#pragma unroll
      for (int t = 0; t < 2; ++t)
#pragma unroll
        for (int cc = 0; cc < 2; ++cc)
          va[t][cc] = (8 * hf + 4 * t + q) * 1024 + (((8 * w + 4 * cc + 2 * blk + (p >> 1)) ^ (4 * q + 2 * (hf ^ t) + hf)) << 4) + 8 * (p & 1);
#pragma unroll
      for (int ks = 0; ks < 8; ++ks)
#pragma unroll
        for (int cc = 0; cc < 2; ++cc) vf[ks][cc] = trfrag(L + va[0][cc] + ks * 16384, L + va[1][cc] + ks * 16384);
      SCHED();
      unsigned todo = 0;
#pragma unroll
      for (int u = 0; u < 8; ++u) if (!__all(TAGOK(g[u]))) todo |= 1u << u;
      for (unsigned spins = 0; todo != 0 && spins < SPIN_CAP; ++spins) {
        __builtin_amdgcn_s_sleep(POLL_SLEEP);
        CBAR();
#pragma unroll
        for (int u = 0; u < 8; ++u) if (todo & (1u << u)) g[u] = LDG(off + u * 8192);
#pragma unroll
        for (int u = 0; u < 8; ++u) if (todo & (1u << u)) { if (__all(TAGOK(g[u]))) todo &= ~(1u << u); }
      }
      const unsigned qb = QO + hw * 1024 + (((gl >> 1) ^ (hw << 2)) << 4) + (gl & 1) * 8;
#pragma unroll
      for (int u = 0; u < 8; ++u) *(LAS v2u*)(L + ((qb ^ (fsw((4 * u) & 15) << 4)) + u * 4096)) = v2u{g[u].y, g[u].w};
      STAMP(2);
    }
    LBAR();
    STAMP(3);
    CYC(0);
    f32x16 S;
#pragma unroll
    for (int i = 0; i < 16; ++i) S[i] = 0.f;
    {
      v4u qf[16];
#pragma unroll
      for (int st = 0; st < 16; ++st) qf[st] = lds128(L + QO + ((ob ^ ((st & 7) << 5)) + (st >> 3) * 256));
      SCHED();
#pragma unroll
      for (int st = 0; st < 16; ++st) S = mfma32(kf[st], qf[st], S);
    }
    CYC(1);
    CYC(2);
    float sv[8];
    if (ch == 0) {
      LAS f32x4* xw = (LAS f32x4*)(L + XCH + w * 2048 + lane * 16);
      xw[0] = f32x4{S[8], S[9], S[10], S[11]}; xw[64] = f32x4{S[12], S[13], S[14], S[15]};
#pragma unroll
      for (int e = 0; e < 8; ++e) sv[e] = S[e];
    } else {
      LAS f32x4* xw = (LAS f32x4*)(L + XCH + w * 2048 + lane * 16);
      xw[0] = f32x4{S[0], S[1], S[2], S[3]}; xw[64] = f32x4{S[4], S[5], S[6], S[7]};
#pragma unroll
      for (int e = 0; e < 8; ++e) sv[e] = S[8 + e];
    }
    LBAR();
    CYC(3);
    {
      LAS f32x4* xr = (LAS f32x4*)(L + XCH + (w ^ 4) * 2048 + lane * 16);
      const f32x4 p0 = xr[0], p1 = xr[64];
#pragma unroll
      for (int e = 0; e < 4; ++e) { sv[e] += p0[e]; sv[4 + e] += p1[e]; }
      float m = fmaxf(fmaxf(fmaxf(sv[0], sv[1]), fmaxf(sv[2], sv[3])), fmaxf(fmaxf(sv[4], sv[5]), fmaxf(sv[6], sv[7])));
      m = max32(m);
      *(LAS float*)(L + MX + r32 * 32 + w * 4) = m;
    }
    LBAR();
    CYC(4);
    float Mx;
    {
      const f32x4 m0 = *(LAS f32x4*)(L + MX + r32 * 32), m1 = *(LAS f32x4*)(L + MX + r32 * 32 + 16);
      Mx = fmaxf(fmaxf(fmaxf(m0[0], m0[1]), fmaxf(m0[2], m0[3])), fmaxf(fmaxf(m1[0], m1[1]), fmaxf(m1[2], m1[3])));
      float l = 0.f;
#pragma unroll
      for (int e = 0; e < 8; ++e) { sv[e] = __builtin_amdgcn_exp2f(sv[e] - Mx); l += sv[e]; }
#pragma unroll
      for (int i = 0; i < 2; ++i)
        *(LAS v2u*)(L + PB + r32 * 256 + (((4 * kt + 2 * ch + i) ^ fr) << 4) + hf * 8) = v2u{pk2(sv[4 * i], sv[4 * i + 1]), pk2(sv[4 * i + 2], sv[4 * i + 3])};
      l = sum32(l);
      *(LAS float*)(L + LS + r32 * 32 + w * 4) = l;
    }
    LBAR();
    CYC(5);
    STAMP(4);
    float Ls;
    f32x16 O0, O1;
#pragma unroll
    for (int i = 0; i < 16; ++i) { O0[i] = 0.f; O1[i] = 0.f; }
    {
      const unsigned pb = PB + r32 * 256 + ((hf ^ fr) << 4);
      v4u pf[8];
#pragma unroll
      for (int ks = 0; ks < 8; ++ks) pf[ks] = lds128(L + (pb ^ (ks << 5)));
      const f32x4 l0 = *(LAS f32x4*)(L + LS + r32 * 32), l1 = *(LAS f32x4*)(L + LS + r32 * 32 + 16);
      SCHED();
#pragma unroll
      for (int ks = 0; ks < 8; ++ks) {
        O0 = mfma32(vf[ks][0], pf[ks], O0);
        O1 = mfma32(vf[ks][1], pf[ks], O1);
      }
      Ls = ((l0[0] + l0[1]) + (l0[2] + l0[3])) + ((l1[0] + l1[1]) + (l1[2] + l1[3]));
    }
    CYC(6);
    {
      LAS char* ot = L + w * 4096;
      const unsigned ow = r32 * 128 + 8 * hf;
#pragma unroll
      for (int gq = 0; gq < 4; ++gq) {
        *(LAS v2u*)(ot + ow + ((gq ^ (r32 & 7)) << 4)) = v2u{pk2(O0[4 * gq], O0[4 * gq + 1]), pk2(O0[4 * gq + 2], O0[4 * gq + 3])};
        *(LAS v2u*)(ot + ow + (((4 + gq) ^ (r32 & 7)) << 4)) = v2u{pk2(O1[4 * gq], O1[4 * gq + 1]), pk2(O1[4 * gq + 2], O1[4 * gq + 3])};
      }
      CYC(7);
      STAMP(5);
      const int go = WS_OPART + (((((lane >> 3) * 4 + (w >> 1)) * 2 + b) * 64 + s) * 256) + (w & 1) * 128 + (((lane & 7) ^ ((lane >> 3) & 7)) << 4);
#pragma unroll
      for (int i = 0; i < 4; ++i) __builtin_amdgcn_raw_buffer_store_b128(lds128(ot + i * 1024 + lane * 16), rs, go + i * (8 * 4 * 2 * 64 * 256), 0, OAUX);
    }
    SCHED();
    CYC(8);
    DRAIN();
    CYC(9);
    if (lane < 32) {
      const v4u gr = {tagA, __float_as_uint(Mx), tagB, __float_as_uint(Ls)};
      STG(gr, WS_STAT + (((b * 64 + s) * 8 + w) * 32 + lane) * 16);
    }
    CYC(10);
    STAMP(6);
    if ((a & 15) < 9) {
      const int r = (a >> 4) * 9 + (a & 15), t2 = tid - 256;
      float g0 = 0.f, g1 = 0.f, bias2 = 0.f;
      if (w == 4) {
        bias2 = bflo((unsigned)b2[8 * r + (lane & 7)]);
        float d0 = 0.f, d1 = 0.f;
#pragma unroll
        for (int i = 0; i < 2; ++i) {
          const int c = lane + 64 * i;
          if (c < 72) {
            const v4u xa = gld(x + c * 8), xb = gld(x + NM + c * 8), gg = gld(gw + c * 8);
#pragma unroll
            for (int e = 0; e < 4; ++e) {
              d0 += bflo(xa[e]) * bflo(gg[e]) + bfhi(xa[e]) * bfhi(gg[e]);
              d1 += bflo(xb[e]) * bflo(gg[e]) + bfhi(xb[e]) * bfhi(gg[e]);
            }
          }
        }
        d0 = wave_sum(d0); d1 = wave_sum(d1);
        g0 = 1.f / (1.f + __expf(-d0)); g1 = 1.f / (1.f + __expf(-d1));
      }
      const int off = WS_POUT + r * 16384 + tid * 16;
      v4u ga[2];
      wait_until(t0 + T_RED);
      for (unsigned spins = 0;; ++spins) {
        CBAR();
#pragma unroll
        for (int i = 0; i < 2; ++i) ga[i] = LDG(off + i * 8192);
        if (__all(TAGOK(ga[0]) & TAGOK(ga[1])) || spins > SPIN_CAP) break;
        __builtin_amdgcn_s_sleep(POLL_SLEEP);
      }
      STAMP(9);
      float v0 = __uint_as_float(ga[0].y) + __uint_as_float(ga[1].y);
      float v1 = __uint_as_float(ga[0].w) + __uint_as_float(ga[1].w);
      v0 += dppf<0x128>(v0); v1 += dppf<0x128>(v1);
      v0 = sum32(sum16(v0)); v1 = sum32(sum16(v1));
      if (lane < 8) { *(LAS float*)(L + 65536 + (lane * 8 + w) * 4) = v0; *(LAS float*)(L + 65536 + 256 + (lane * 8 + w) * 4) = v1; }
      LBAR();
      if (t2 >= 0 && t2 < 16) {
        const f32x4 p0 = *(LAS f32x4*)(L + 65536 + t2 * 32), p1 = *(LAS f32x4*)(L + 65536 + t2 * 32 + 16);
        const float v = ((p0[0] + p0[1]) + (p0[2] + p0[3])) + ((p1[0] + p1[1]) + (p1[2] + p1[3])) + (1.f - (t2 < 8 ? g0 : g1)) * bias2;
        out[(t2 >> 3) * NM + 8 * r + (t2 & 7)] = (u16)bf1(v);
      }
      if (r == 0 && tid == 0) __hip_atomic_store((GAS u64*)(ws + WS_EPOCH), epoch, __ATOMIC_RELAXED, __HIP_MEMORY_SCOPE_AGENT);
      STAMP(10);
    }
    STAMP_FLUSH();
  }
}

extern "C" void kernel_launch(void* const* d_in, const int* in_sizes, int n_in, void* d_out, int out_size, void* d_ws, size_t ws_size,
                              hipStream_t stream) {
  fused_kernel<<<256, 512, 0, stream>>>((const u16*)d_in[0], (const u16*)d_in[1], (const u16*)d_in[2], (const u16*)d_in[3],
                                        (const u16*)d_in[4], (const u16*)d_in[5], (const u16*)d_in[7], (const u16*)d_in[8],
                                        (const u16*)d_in[9], (const u16*)d_in[10], (const u16*)d_in[11], (u16*)d_out, (char*)d_ws + WS_BASE);
}
```

```cpp
#include <hip/hip_runtime.h>
#include <hip/hip_bf16.h>
#include <stdint.h>

#define GAS __attribute__((address_space(1)))
#define LAS __attribute__((address_space(3)))
#define DI __device__ __forceinline__
typedef unsigned short u16;
typedef unsigned long long u64;
typedef unsigned v4u __attribute__((ext_vector_type(4)));
typedef unsigned v2u __attribute__((ext_vector_type(2)));
typedef float f32x2 __attribute__((ext_vector_type(2)));
typedef float f32x4 __attribute__((ext_vector_type(4)));
typedef float f32x16 __attribute__((ext_vector_type(16)));
typedef short bf16x8 __attribute__((ext_vector_type(8)));
typedef short s16x4 __attribute__((ext_vector_type(4)));
typedef __bf16 bf2_t __attribute__((ext_vector_type(2)));

constexpr int NM = 576, NH = 32, ND = 128, NC = 512, NSK = 8192, NF = 4608;
constexpr int WS_EPOCH = 0;
constexpr int WS_HX = 4096;
constexpr int WS_QX = WS_HX + 4 * 73728;
constexpr int WS_CX = WS_QX + 262144;
constexpr int WS_QABS = WS_CX + 262144;
constexpr int WS_STAT = WS_QABS + 131072;
constexpr int WS_POUT = WS_STAT + 524288;
constexpr int WS_OPART = WS_POUT + 72 * 128 * 128;
constexpr int WS_STAMP = WS_OPART + 4194304;
constexpr int WS_BYTES = WS_STAMP + 2 * 256 * 256;
constexpr int AUX_SC1 = 16;
constexpr unsigned SPIN_CAP = 1u << 17;

#define CKV_N1 0
#define CKV_WIN 2
#define CKV_WIN2 16
#define PRETOUCH 0
#define XPOL ""
#define STAT_SW 1
#define STAT_SLEEP 2
#define OAUX 16
#define OPRE 2
#define WS_BASE 0
#define BULK_WIN 32
#define PPRE 1
#define HO0_SLEEP 3
#define T_CKV 150
#define POLL_SLEEP 2
#define T_STATS 850
#define T_HEADB 470
#define T_RED 800

#define STAMP(i) do {} while (0)
#define STAMPW(wv, i) do {} while (0)
#define STAMP_FLUSH() do {} while (0)
#define CYC(i) do {} while (0)

DI unsigned fsw(unsigned r) { return ((r & 3u) << 2) | ((((r >> 3) ^ (r >> 2)) & 1u) << 1) | ((r >> 3) & 1u); }
DI float bflo(unsigned v) { return __uint_as_float(v << 16); }
DI float bfhi(unsigned v) { return __uint_as_float(v & 0xffff0000u); }
DI unsigned pk2(float a, float b) { f32x2 f = {a, b}; bf2_t r = __builtin_convertvector(f, bf2_t); return __builtin_bit_cast(unsigned, r); }
DI unsigned bf1(float a) { return pk2(a, 0.f) & 0xffffu; }
DI f32x4 mfma16(v4u a, v4u b, f32x4 c) { return __builtin_amdgcn_mfma_f32_16x16x32_bf16(__builtin_bit_cast(bf16x8, a), __builtin_bit_cast(bf16x8, b), c, 0, 0, 0); }
DI f32x16 mfma32(v4u a, v4u b, f32x16 c) { return __builtin_amdgcn_mfma_f32_32x32x16_bf16(__builtin_bit_cast(bf16x8, a), __builtin_bit_cast(bf16x8, b), c, 0, 0, 0); }
DI v2u trread(LAS char* p) { s16x4 r = __builtin_amdgcn_ds_read_tr16_b64_v4i16((LAS s16x4*)p); return __builtin_bit_cast(v2u, r); }
DI v4u trfrag(LAS char* p0, LAS char* p1) { v2u a = trread(p0), b = trread(p1); return v4u{a.x, a.y, b.x, b.y}; }
DI v4u lds128(LAS char* p) { return *(LAS v4u*)p; }
DI void dma16(const u16* g, LAS char* l) { __builtin_amdgcn_global_load_lds((const GAS unsigned*)g, (LAS unsigned*)l, 16, 0, 2); }
DI v4u gld(const u16* p) { return *(const GAS v4u*)p; }
DI v4u gldnt(const u16* p) { return __builtin_nontemporal_load((const GAS v4u*)p); }
DI v2u gldnt8(const u16* p) { return __builtin_nontemporal_load((const GAS v2u*)p); }
template <int CTRL> DI float dppf(float x) { return __builtin_bit_cast(float, __builtin_amdgcn_mov_dpp(__builtin_bit_cast(int, x), CTRL, 0xf, 0xf, true)); }
template <int CTRL> DI unsigned dppu(unsigned x) { return (unsigned)__builtin_amdgcn_mov_dpp((int)x, CTRL, 0xf, 0xf, true); }
DI float sum32(float x) { auto t = __builtin_amdgcn_permlane32_swap(__float_as_uint(x), __float_as_uint(x), false, false); return __uint_as_float(t[0]) + __uint_as_float(t[1]); }
DI float max32(float x) { auto t = __builtin_amdgcn_permlane32_swap(__float_as_uint(x), __float_as_uint(x), false, false); return fmaxf(__uint_as_float(t[0]), __uint_as_float(t[1])); }
DI float sum16(float x) { auto t = __builtin_amdgcn_permlane16_swap(__float_as_uint(x), __float_as_uint(x), false, false); return __uint_as_float(t[0]) + __uint_as_float(t[1]); }
DI float max16(float x) { auto t = __builtin_amdgcn_permlane16_swap(__float_as_uint(x), __float_as_uint(x), false, false); return fmaxf(__uint_as_float(t[0]), __uint_as_float(t[1])); }
DI float wave_sum(float x) { x += dppf<0xB1>(x); x += dppf<0x4E>(x); x += dppf<0x141>(x); x += dppf<0x128>(x); x = sum16(x); return sum32(x); }
DI float wave_max(float x) { x = fmaxf(x, dppf<0xB1>(x)); x = fmaxf(x, dppf<0x4E>(x)); x = fmaxf(x, dppf<0x141>(x)); x = fmaxf(x, dppf<0x128>(x)); x = max16(x); return max32(x); }
DI void wait_until(u64 t) { while (__builtin_amdgcn_s_memrealtime() < t) __builtin_amdgcn_s_sleep(1); }
#define DRAIN() asm volatile("s_waitcnt vmcnt(0)" ::: "memory")
#define CBAR() asm volatile("" ::: "memory")
#define SCHED() __builtin_amdgcn_sched_barrier(0)
#define LBAR() asm volatile("s_waitcnt lgkmcnt(0)\n\ts_barrier" ::: "memory")
#define DMA16(gp, ldsoff) do { unsigned keep_; asm volatile("s_mov_b32 %0, m0\n\ts_mov_b32 m0, %2\n\ts_nop 0\n\tglobal_load_lds_dwordx4 %1, off nt\n\ts_mov_b32 m0, %0" : "=&s"(keep_) : "v"(gp), "s"((unsigned)(size_t)L + (unsigned)(ldsoff)) : "memory"); } while (0)
#define TOUCH(dst, off) asm volatile("buffer_load_dword %0, %1, %2, 0 offen sc1" : "=&v"(dst) : "v"(off), "s"(rs) : "memory")
#define DMA16X(gp, ldsoff) do { unsigned keep_; asm volatile("s_mov_b32 %0, m0\n\ts_mov_b32 m0, %2\n\ts_nop 0\n\tglobal_load_lds_dwordx4 %1, off" XPOL "\n\ts_mov_b32 m0, %0" : "=&s"(keep_) : "v"(gp), "s"((unsigned)(size_t)L + (unsigned)(ldsoff)) : "memory"); } while (0)
#define DMA16W(gp, ldsoff) do { DMA16(gp, ldsoff); if (BULK_WIN < 32) asm volatile("s_waitcnt vmcnt(%0)" :: "i"(BULK_WIN - 1) : "memory"); } while (0)
#define LDG(off) __builtin_amdgcn_raw_buffer_load_b128(rs, (off), 0, AUX_SC1)
#define STG(v, off) __builtin_amdgcn_raw_buffer_store_b128((v), rs, (off), 0, AUX_SC1)
#define TAGOK(g) (((g).x == tagA) & ((g).z == tagB))

DI int tile_lane_off(int lane, int ld) { const int r8 = lane >> 3; return (8 * (r8 >> 2) + (r8 & 3)) * ld + 8 * ((lane & 7) ^ ((r8 >> 1) << 1)); }
DI int tile_tr_off(int lane, int ct) { const int r8 = 4 * ((lane >> 4) & 1) + ((lane >> 2) & 3); return (lane >> 5) * 1024 + r8 * 128 + ((ct ^ (r8 >> 1)) * 32) + (lane & 3) * 8; }
DI void put_hilo(LAS char* base, int stride, int d, float v0, float v1) {
  const unsigned h = pk2(v0, v1);
  const unsigned l = pk2(v0 - bflo(h), v1 - bfhi(h));
  *(LAS u16*)(base + d * 2) = (u16)h;
  *(LAS u16*)(base + stride + d * 2) = (u16)(h >> 16);
  *(LAS u16*)(base + 2 * stride + d * 2) = (u16)l;
  *(LAS u16*)(base + 3 * stride + d * 2) = (u16)(l >> 16);
}

__global__ void __launch_bounds__(512, 2)
fused_kernel(const u16* __restrict__ x, const u16* __restrict__ ckv, const u16* __restrict__ wq, const u16* __restrict__ wuk,
             const u16* __restrict__ wuv, const u16* __restrict__ wo, const u16* __restrict__ w1, const u16* __restrict__ b1,
             const u16* __restrict__ w2, const u16* __restrict__ b2, const u16* __restrict__ gw, u16* __restrict__ out, char* ws) {
  __shared__ __attribute__((aligned(16))) char smem[163840];
  LAS char* L = (LAS char*)smem;
  const int tid = threadIdx.x, lane = tid & 63, w = __builtin_amdgcn_readfirstlane(tid >> 6);
  const int bid = blockIdx.x, xcd = bid & 7, kk = bid >> 3;
  const u64 t0 = __builtin_amdgcn_s_memrealtime();
  const __amdgpu_buffer_rsrc_t rs = __builtin_amdgcn_make_buffer_rsrc((void*)ws, 0, WS_BYTES, 0x00020000);
#define LOAD_EPOCH() (__hip_atomic_load((GAS u64*)(ws + WS_EPOCH), __ATOMIC_RELAXED, __HIP_MEMORY_SCOPE_AGENT) + 1ull)
  STAMP(0);
  const int g4 = lane >> 4, col = lane & 15;

  if (kk < 16) {
    const int h = xcd * 4 + (kk >> 2), j = kk & 3;
    constexpr int WQ = 0, WUK = 40960, W1 = 69632, W2 = W1 + 36864, W2X = 143360, WUV = 0, WO = 32768;
    constexpr int RED = 152576, CTXA = RED + 4096, QA = CTXA + 1024, PRIV = QA + 1024, HR = PRIV + 2048;
    const int ks0 = j < 2 ? 5 * j : 4 * j + 2;
    u64 epoch;
    asm volatile("global_load_dwordx2 %0, %1, off sc1" : "=&v"(epoch) : "v"(ws + WS_EPOCH) : "memory");
    constexpr int XG = W2X;
    if (w < 3) { const int c = 64 * w + lane; DMA16X(x + 8 * (c < 144 ? c : 0), XG + w * 1024); }
    else if (w < 5) { const int c = 64 * (w - 3) + lane; DMA16X(gw + 8 * (c < 72 ? c : 0), XG + w * 1024); }
    {
      const u16* src = wq + (size_t)(32 * ks0) * 4096 + h * ND + tile_lane_off(lane, 4096) + (16 * (w & 1) + 4 * (w >> 2)) * 4096 + 64 * ((w >> 1) & 1);
#pragma unroll
      for (int i = 0; i < 5; ++i)
        if (i < 4 || j < 2) DMA16(src + (size_t)i * 32 * 4096, WQ + (i * 8 + w) * 1024);
#pragma unroll
      for (int i = 0; i < 4; ++i) {
        const int row = 16 * w + 4 * i + g4;
        DMA16(wuk + (size_t)(128 * j + row) * 4096 + h * ND + 8 * (col ^ fsw(4 * i + g4)), WUK + (4 * w + i) * 1024);
      }
    }
    asm volatile("s_waitcnt vmcnt(4)\n\ts_barrier" : "+v"(epoch) :: "memory");
    v4u xa[5];
#pragma unroll
    for (int i = 0; i < 5; ++i) xa[i] = lds128(L + XG + (col & 1) * 1152 + 64 * (ks0 + ((i < 4 || j < 2) ? i : 0)) + 16 * g4);
    epoch += 1ull;
    if ((unsigned)epoch == 0u) epoch += 1ull;
    const unsigned tagA = (unsigned)epoch, tagB = (unsigned)(epoch >> 32);
    STAMP(1);
    {
      f32x4 acc = {0.f, 0.f, 0.f, 0.f};
      LAS char* p = L + WQ + (w >> 2) * 2048 + tile_tr_off(lane, w & 3);
      v4u bq[5];
#pragma unroll
      for (int i = 0; i < 4; ++i) bq[i] = trfrag(p + i * 8192, p + i * 8192 + 4096);
      if (j < 2) bq[4] = trfrag(p + 4 * 8192, p + 4 * 8192 + 4096);
#pragma unroll
      for (int i = 0; i < 4; ++i) acc = mfma16(xa[i], bq[i], acc);
      if (j < 2) acc = mfma16(xa[4], bq[4], acc);
      if (g4 == 0) { const v4u gr = {tagA, __float_as_uint(acc[0]), tagB, __float_as_uint(acc[1])}; STG(gr, WS_QX + ((h * 4 + j) * 128 + 16 * w + col) * 16); }
    }
    STAMP(2);
    float g0, g1;
    {
      const int gc = lane + 64 < 72 ? lane + 64 : 0;
      const v4u gq[6] = {lds128(L + XG + lane * 16), lds128(L + XG + 1152 + lane * 16), lds128(L + XG + 3072 + lane * 16),
                         lds128(L + XG + gc * 16), lds128(L + XG + 1152 + gc * 16), lds128(L + XG + 3072 + gc * 16)};
      float d0 = 0.f, d1 = 0.f, e0 = 0.f, e1 = 0.f;
#pragma unroll
      for (int e = 0; e < 4; ++e) {
        d0 += bflo(gq[0][e]) * bflo(gq[2][e]) + bfhi(gq[0][e]) * bfhi(gq[2][e]);
        d1 += bflo(gq[1][e]) * bflo(gq[2][e]) + bfhi(gq[1][e]) * bfhi(gq[2][e]);
        e0 += bflo(gq[3][e]) * bflo(gq[5][e]) + bfhi(gq[3][e]) * bfhi(gq[5][e]);
        e1 += bflo(gq[4][e]) * bflo(gq[5][e]) + bfhi(gq[4][e]) * bfhi(gq[5][e]);
      }
      if (lane < 8) { d0 += e0; d1 += e1; }
      d0 = wave_sum(d0); d1 = wave_sum(d1);
      g0 = 1.f / (1.f + __expf(-d0)); g1 = 1.f / (1.f + __expf(-d1));
      asm volatile("" : "+v"(g0), "+v"(g1));
    }
    DRAIN();
    __builtin_amdgcn_s_sleep(HO0_SLEEP);
    LAS char* pv = L + PRIV + w * 256;
    {
      v4u ga;
      const int off = WS_QX + ((h * 4 + g4) * 128 + 16 * w + col) * 16;
      for (unsigned spins = 0;; ++spins) {
        CBAR();
        ga = LDG(off);
        if (__all(TAGOK(ga)) || spins > SPIN_CAP) break;
        __builtin_amdgcn_s_sleep(1);
      }
      STAMP(3);
      v4u uk[4];
#pragma unroll
      for (int ks = 0; ks < 4; ++ks) uk[ks] = lds128(L + WUK + (16 * w + col) * 256 + (((4 * ks + g4) ^ fsw(col)) << 4));
      const float q0 = sum32(sum16(__uint_as_float(ga.y))), q1 = sum32(sum16(__uint_as_float(ga.w)));
      const float qv = (g4 & 1) ? q1 : q0;
      const unsigned hi = bf1(qv);
      *(LAS u16*)(L + QA + g4 * 256 + (16 * w + col) * 2) = (u16)((g4 & 2) ? bf1(qv - bflo(hi)) : hi);
      LBAR();
      f32x4 a = {0.f, 0.f, 0.f, 0.f};
#pragma unroll
      for (int ks = 0; ks < 4; ++ks) a = mfma16(lds128(L + QA + (col & 3) * 256 + ks * 64 + g4 * 16), uk[ks], a);
      const float v = ((g4 & 1) ? (a[1] + a[3]) : (a[0] + a[2])) * (0.08838834764831845f * 1.4426950408889634f);
      const unsigned bits = bf1(v);
      const unsigned n1 = dppu<0x55>(bits), n2 = dppu<0xAA>(bits), n3 = dppu<0xFF>(bits);
      if ((lane & 3) == 0 && g4 < 2) {
        const v4u gr = {tagA, bits | (n1 << 16), tagB, n2 | (n3 << 16)};
        STG(gr, WS_QABS + ((g4 * 32 + h) * 128 + 32 * j + 4 * w + (col >> 2)) * 16);
      }
    }
    STAMP(4);
    const int pidx = h * 4 + j;
    const bool dbl = kk >= 14;
    const int u0 = dbl ? 2 * kk - 14 : kk;
    const int r2 = dbl ? 4480 + 8 * (xcd * 2 + kk - 14) : 40 * (xcd * 14 + kk);
    const int nk2 = dbl ? 8 : 40;
    unsigned b1v[3];
    {
      v4u xm[2][5];
      const int sub = w >> 2, kb = 5 * sub;
#pragma unroll
      for (int i = 0; i < 5; ++i) {
        const int ki = kb + ((i < 4 || sub == 0) ? i : 0);
        xm[0][i] = lds128(L + XG + (col & 1) * 1152 + 64 * (9 * (u0 & 1) + ki) + 16 * g4);
        xm[1][i] = lds128(L + XG + (col & 1) * 1152 + 64 * (9 + ki) + 16 * g4);
      }
      LBAR();
      if (T_HEADB > 0) wait_until(t0 + T_HEADB);
      {
        const u16* src = wuv + (size_t)(128 * j) * 4096 + h * ND + tile_lane_off(lane, 4096) + (16 * (w & 1) + 4 * (w >> 2)) * 4096 + 64 * ((w >> 1) & 1);
#pragma unroll
        for (int i = 0; i < 4; ++i) DMA16W(src + (size_t)i * 32 * 4096, WUV + (i * 8 + w) * 1024);
        const u16* so = wo + (size_t)(h * ND + 32 * j) * NM + tile_lane_off(lane, NM) + (16 * (w & 1) + 4 * ((w >> 1) & 1)) * NM + 64 * (w >> 2);
#pragma unroll
        for (int i = 0; i < 5; ++i)
          if (i < 4 || w < 4) DMA16W(so + 128 * i, WO + (i * 8 + w) * 1024);
      }
      {
        const int r8 = lane >> 3;
        const u16* s3 = w2 + (size_t)(r2 + nk2 - 8 + r8) * NM + 8 * ((lane & 7) ^ ((r8 >> 1) << 1));
        DMA16W(s3 + 64 * w, W2X + w * 1024);
        if (w == 0) DMA16W(s3 + 512, W2X + 8192);
      }
      if (!dbl) {
        const u16* s2 = w2 + (size_t)r2 * NM + tile_lane_off(lane, NM) + (16 * (w & 1) + 4 * ((w >> 1) & 1)) * NM + 64 * (w >> 2);
#pragma unroll
        for (int i = 0; i < 5; ++i)
          if (i < 4 || w < 4) DMA16W(s2 + 128 * i, W2 + (i * 8 + w) * 1024);
      }
      {
        const int fb = r2 + col;
        asm volatile("global_load_ushort %0, %3, off\n\tglobal_load_ushort %1, %4, off\n\tglobal_load_ushort %2, %5, off"
                     : "=&v"(b1v[0]), "=&v"(b1v[1]), "=&v"(b1v[2])
                     : "v"(b1 + (col < nk2 ? fb : r2)), "v"(b1 + (16 + col < nk2 ? fb + 16 : r2)), "v"(b1 + (32 + col < nk2 ? fb + 32 : r2)) : "memory");
      }
      const int f0 = 64 * (xcd * 9 + (u0 >> 1));
      {
        const u16* s1 = w1 + f0 + tile_lane_off(lane, NF) + (size_t)(288 * (u0 & 1) + 32 * (w >> 2) + 16 * (w & 1) + 4 * ((w >> 1) & 1)) * NF;
#pragma unroll
        for (int i = 0; i < 5; ++i)
          if (i < 4 || w < 4) DMA16W(s1 + (size_t)i * 64 * NF, W1 + (i * 8 + w) * 1024);
        if (dbl) {
#pragma unroll
          for (int i = 0; i < 5; ++i)
            if (i < 4 || w < 4) DMA16W(s1 + (size_t)(288 + i * 64) * NF, W2 + (i * 8 + w) * 1024);
        }
      }
      asm volatile("s_waitcnt vmcnt(0)\n\ts_barrier" : "+v"(b1v[0]), "+v"(b1v[1]), "+v"(b1v[2]) :: "memory");
      STAMP(10);
#pragma unroll
      for (int un = 0; un < 2; ++un) {
        if (un == 0 || dbl) {
          LAS char* p = L + W1 + un * 36864 + kb * 4096 + tile_tr_off(lane, w & 3);
          f32x4 acc = {0.f, 0.f, 0.f, 0.f};
#pragma unroll
          for (int i = 0; i < 4; ++i) acc = mfma16(xm[un][i], trfrag(p + i * 4096, p + i * 4096 + 2048), acc);
          if (sub == 0) acc = mfma16(xm[un][4], trfrag(p + 4 * 4096, p + 4 * 4096 + 2048), acc);
          if (g4 == 0) {
            const v4u gr = {tagA, __float_as_uint(acc[0]), tagB, __float_as_uint(acc[1])};
            STG(gr, WS_HX + ((((u0 & 1) + un) * 2 + sub) * NF + f0 + 16 * (w & 3) + col) * 16);
          }
        }
      }
      STAMP(11);
    }
    v4u uv[4], wf[5];
    {
      LAS char* p = L + WUV + (w >> 2) * 2048 + tile_tr_off(lane, w & 3);
#pragma unroll
      for (int i = 0; i < 4; ++i) uv[i] = trfrag(p + i * 8192, p + i * 8192 + 4096);
      LAS char* po = L + WO + (w >> 2) * 4096 + tile_tr_off(lane, w & 3);
#pragma unroll
      for (int i = 0; i < 5; ++i) wf[i] = (i < 4 || w < 4) ? trfrag(po + i * 8192, po + i * 8192 + 2048) : v4u{0u, 0u, 0u, 0u};
    }
    v4u w2m[5], w2x[5];
    {
      const int q = (lane >> 2) & 3;
      LAS char* p = L + W2X + (w >> 2) * 1024 + q * 128 + (lane & 3) * 8;
      const int o0 = (((w & 3) ^ (q >> 1)) * 32), o1 = 512 + (((w & 3) ^ (2 + (q >> 1))) * 32);
      LAS char* pm = L + W2 + (w >> 2) * 4096 + tile_tr_off(lane, w & 3);
#pragma unroll
      for (int i = 0; i < 5; ++i) {
        const int ii = (i < 4 || w < 4) ? i : 0;
        w2x[i] = trfrag(p + ii * 2048 + o0, p + ii * 2048 + o1);
        w2m[i] = trfrag(pm + ii * 8192, pm + ii * 8192 + 2048);
      }
    }
    STAMP(5);
    const int b = w >> 2, q4 = w & 3;
    float wgt;
    v4u hg = {0u, 0u, 0u, 0u}, cxw = {0u, 0u, 0u, 0u};
    const int ht = w - 2, hf_ = 16 * ht + col;
    const int ho = WS_HX + (g4 * NF + r2 + (hf_ < nk2 ? hf_ : 0)) * 16;
    constexpr int HA = QA;
    {
      const int off = WS_STAT + (((b * 64 + 16 * q4 + ((lane >> 1) & 15)) * 8 + 2 * j + (lane & 1)) * 32 + h) * 16;
      const int ooff = WS_OPART + ((((h * 4 + j) * 2 + b) * 64 + 16 * q4) * 256) + lane * 16;
      v4u g, o[4];
      bool ok = lane >= 32;
      unsigned done = 0;
      {
        const v4u z = {0u, 0u, 0u, 0u};
        const int ct = w + 8 * g4;
        STG(z, WS_POUT + ((2 * ct + (col >> 3)) * 128 + pidx) * 128 + (col & 7) * 16);
        if (w < 4 && g4 == 0) STG(z, WS_POUT + ((2 * (w + 32) + (col >> 3)) * 128 + pidx) * 128 + (col & 7) * 16);
      }
      if (w < 2) { cxw = LDG(WS_CX + ((h * 4 + j) * 128 + w * 64 + lane) * 16); asm volatile("" :: "v"(cxw)); }
      wait_until(t0 + T_STATS);
      for (unsigned spins = 0;; ++spins) {
        CBAR();
        if (!ok) { g = LDG(off); ok = TAGOK(g); }
        const unsigned m = (unsigned)__builtin_amdgcn_ballot_w64(ok) | (spins > SPIN_CAP ? 0xffffffffu : 0u);
        __builtin_amdgcn_fence(__ATOMIC_ACQUIRE, "wavefront");
#pragma unroll
        for (int i = 0; i < 4; ++i)
          if (!(done & (1u << i)) && ((m >> (8 * i)) & 0xffu) == 0xffu) { o[i] = LDG(ooff + i * 1024); done |= 1u << i; }
        if (done == 15u) break;
        __builtin_amdgcn_s_sleep(STAT_SLEEP);
      }
      if (ht >= 0 && ht < 3) hg = LDG(ho);
      STAMPW(0, 6);
      const float ms = lane < 32 ? __uint_as_float(g.y) : -INFINITY, ls = (lane < 32 && !(lane & 1)) ? __uint_as_float(g.w) : 0.f;
      float Mw = fmaxf(ms, dppf<0x4E>(ms));
      Mw = fmaxf(Mw, dppf<0x141>(Mw)); Mw = fmaxf(Mw, dppf<0x128>(Mw)); Mw = max32(max16(Mw));
      wgt = __builtin_amdgcn_exp2f(ms - Mw);
      float ww[4];
#pragma unroll
      for (int i = 0; i < 4; ++i) ww[i] = __uint_as_float((unsigned)__builtin_amdgcn_ds_bpermute((4 * i + g4) * 8, (int)__float_as_uint(wgt)));
      float Lw = wgt * ls;
      Lw += dppf<0xB1>(Lw); Lw += dppf<0x4E>(Lw); Lw += dppf<0x141>(Lw); Lw += dppf<0x128>(Lw); Lw = sum32(sum16(Lw));
      if (lane == 0) *(LAS f32x2*)(L + HR + w * 8) = f32x2{Mw, Lw};
      asm volatile("" : "+v"(ww[0]), "+v"(ww[1]), "+v"(ww[2]), "+v"(ww[3]));
      SCHED();
      float acc[8] = {0.f, 0.f, 0.f, 0.f, 0.f, 0.f, 0.f, 0.f};
#pragma unroll
      for (int i = 0; i < 4; ++i) {
        acc[0] += ww[i] * bflo(o[i].x); acc[1] += ww[i] * bfhi(o[i].x);
        acc[2] += ww[i] * bflo(o[i].y); acc[3] += ww[i] * bfhi(o[i].y);
        acc[4] += ww[i] * bflo(o[i].z); acc[5] += ww[i] * bfhi(o[i].z);
        acc[6] += ww[i] * bflo(o[i].w); acc[7] += ww[i] * bfhi(o[i].w);
      }
      float r4[4];
#pragma unroll
      for (int e = 0; e < 4; ++e) {
        auto t = __builtin_amdgcn_permlane32_swap(__float_as_uint(acc[e]), __float_as_uint(acc[4 + e]), false, false);
        r4[e] = sum16(__uint_as_float(t[0]) + __uint_as_float(t[1]));
      }
      if ((g4 & 1) == 0) *(LAS f32x4*)(L + RED + w * 512 + col * 32 + (lane >> 5) * 16) = f32x4{r4[0], r4[1], r4[2], r4[3]};
    }
    LBAR();
    if (tid < 128) {
      LAS float* r = (LAS float*)(L + RED) + tid;
      const f32x4 s0 = *(LAS f32x4*)(L + HR), s1 = *(LAS f32x4*)(L + HR + 16), s2 = *(LAS f32x4*)(L + HR + 32), s3 = *(LAS f32x4*)(L + HR + 48);
      const float Ma = fmaxf(fmaxf(s0[0], s0[2]), fmaxf(s1[0], s1[2])), Mb = fmaxf(fmaxf(s2[0], s2[2]), fmaxf(s3[0], s3[2]));
      const float a0 = __builtin_amdgcn_exp2f(s0[0] - Ma), a1 = __builtin_amdgcn_exp2f(s0[2] - Ma), a2 = __builtin_amdgcn_exp2f(s1[0] - Ma), a3 = __builtin_amdgcn_exp2f(s1[2] - Ma);
      const float c0 = __builtin_amdgcn_exp2f(s2[0] - Mb), c1 = __builtin_amdgcn_exp2f(s2[2] - Mb), c2 = __builtin_amdgcn_exp2f(s3[0] - Mb), c3 = __builtin_amdgcn_exp2f(s3[2] - Mb);
      const float ia = __builtin_amdgcn_rcpf((a0 * s0[1] + a1 * s0[3]) + (a2 * s1[1] + a3 * s1[3]));
      const float ib = __builtin_amdgcn_rcpf((c0 * s2[1] + c1 * s2[3]) + (c2 * s3[1] + c3 * s3[3]));
      put_hilo(L + CTXA, 256, tid, ((a0 * r[0] + a1 * r[128]) + (a2 * r[256] + a3 * r[384])) * ia, ((c0 * r[512] + c1 * r[640]) + (c2 * r[768] + c3 * r[896])) * ib);
    } else if (w < 5) {
      for (unsigned spins = 0; !__all(TAGOK(hg)) && spins < SPIN_CAP; ++spins) {
        __builtin_amdgcn_s_sleep(POLL_SLEEP);
        CBAR();
        hg = LDG(ho);
      }
      const float bb = bflo(ht == 0 ? b1v[0] : ht == 1 ? b1v[1] : b1v[2]);
      const float p0 = fmaxf(sum32(sum16(__uint_as_float(hg.y))) + bb, 0.f), p1 = fmaxf(sum32(sum16(__uint_as_float(hg.w))) + bb, 0.f);
      const float hv = hf_ < nk2 ? ((g4 & 1) ? p1 * p1 : p0 * p0) : 0.f;
      const unsigned hi = bf1(hv);
      const int kpos = dbl ? 32 - 16 * ht + col : hf_;
      *(LAS u16*)(L + HA + g4 * 128 + kpos * 2) = (u16)((g4 & 2) ? bf1(hv - bflo(hi)) : hi);
    } else if (w == 5) {
      *(LAS u16*)(L + HA + g4 * 128 + (48 + col) * 2) = 0;
    }
    LBAR();
    {
      f32x4 acc = {0.f, 0.f, 0.f, 0.f};
#pragma unroll
      for (int i = 0; i < 4; ++i) acc = mfma16(lds128(L + CTXA + (col & 3) * 256 + i * 64 + g4 * 16), uv[i], acc);
      if (g4 == 0) {
        const v4u gr = {tagA, __float_as_uint(acc[0] + acc[2]), tagB, __float_as_uint(acc[1] + acc[3])};
        STG(gr, WS_CX + (((h * 4 + (w >> 1)) * 4 + j) * 32 + 16 * (w & 1) + col) * 16);
      }
    }
    STAMP(7);
    f32x2 mpa, mpb;
    {
      const v4u ha0 = lds128(L + HA + (col & 3) * 128 + g4 * 16), hax = lds128(L + HA + (col & 3) * 128 + 64 + g4 * 16);
      f32x4 acc[5];
#pragma unroll
      for (int i = 0; i < 5; ++i) acc[i] = mfma16(ha0, w2m[i], mfma16(hax, w2x[i], f32x4{0.f, 0.f, 0.f, 0.f}));
      const f32x4 a = g4 == 0 ? acc[0] : g4 == 1 ? acc[1] : g4 == 2 ? acc[2] : acc[3];
      mpa = f32x2{(1.f - g0) * (a[0] + a[2]), (1.f - g1) * (a[1] + a[3])};
      mpb = f32x2{(1.f - g0) * (acc[4][0] + acc[4][2]), (1.f - g1) * (acc[4][1] + acc[4][3])};
    }
    {
      v4u ga, gb;
      const int off = WS_CX + ((h * 4 + j) * 128 + lane) * 16;
      for (unsigned spins = 0;; ++spins) {
        CBAR();
        ga = LDG(off); gb = LDG(off + 1024);
        if (__all(TAGOK(ga) & TAGOK(gb)) || spins > SPIN_CAP) break;
        __builtin_amdgcn_s_sleep(1);
      }
      STAMP(8);
      const float v0 = sum32(__uint_as_float(ga.y) + __uint_as_float(gb.y)), v1 = sum32(__uint_as_float(ga.w) + __uint_as_float(gb.w));
      if (lane < 32) put_hilo(pv, 64, lane, v0, v1);
      const v4u ca = lds128(pv + (col & 3) * 64 + g4 * 16);
      f32x4 acc[5];
#pragma unroll
      for (int i = 0; i < 5; ++i) acc[i] = mfma16(ca, wf[i], f32x4{0.f, 0.f, 0.f, 0.f});
      const f32x4 a = g4 == 0 ? acc[0] : g4 == 1 ? acc[1] : g4 == 2 ? acc[2] : acc[3];
      {
        const int ct = w + 8 * g4;
        const f32x2 mp = mpa;
        const v4u gr = {tagA, __float_as_uint(g0 * (a[0] + a[2]) + mp.x), tagB, __float_as_uint(g1 * (a[1] + a[3]) + mp.y)};
        STG(gr, WS_POUT + ((2 * ct + (col >> 3)) * 128 + pidx) * 128 + (col & 7) * 16);
      }
      if (w < 4 && g4 == 0) {
        const int ct = w + 32;
        const f32x2 mp = mpb;
        const v4u gr = {tagA, __float_as_uint(g0 * (acc[4][0] + acc[4][2]) + mp.x), tagB, __float_as_uint(g1 * (acc[4][1] + acc[4][3]) + mp.y)};
        STG(gr, WS_POUT + ((2 * ct + (col >> 3)) * 128 + pidx) * 128 + (col & 7) * 16);
      }
    }
    STAMP(9);
    STAMP_FLUSH();
    __builtin_amdgcn_endpgm();
  } else {
    const int a = xcd * 16 + (kk - 16), b = a >> 6, s = a & 63;
    constexpr int QO = 131072, XCH = 32768, PB = XCH + 16384, MX = XCH + 24576, LS = XCH + 25600;
    u64 epoch;
    asm volatile("global_load_dwordx2 %0, %1, off sc1" : "=&v"(epoch) : "v"(ws + WS_EPOCH) : "memory");
    {
      const u16* src = ckv + ((size_t)b * NSK + 128 * s + 16 * w) * NC;
#pragma unroll
      for (int i = 0; i < 16; ++i) {
        if (i == CKV_N1) wait_until(t0 + T_CKV);
        DMA16(src + i * NC + ((lane ^ fsw(i)) * 8), (16 * w + i) * 1024);
        if (i < CKV_N1 && i >= CKV_WIN - 1) asm volatile("s_waitcnt vmcnt(%0)" :: "i"(CKV_WIN - 1) : "memory");
        if (i >= CKV_N1 && i < 15 && CKV_WIN2 < 16) asm volatile("s_waitcnt vmcnt(%0)" :: "i"(CKV_WIN2 - 1) : "memory");
      }
    }
    const int r32 = lane & 31, hf = lane >> 5, kt = w & 3, ch = w >> 2;
    const unsigned fr = fsw(r32 & 15);
    const unsigned ob = r32 * 1024 + ch * 512 + ((hf ^ fr) << 4);
    asm volatile("s_waitcnt vmcnt(0)\n\ts_barrier" : "+v"(epoch) :: "memory");
    epoch += 1ull;
    if ((unsigned)epoch == 0u) epoch += 1ull;
    const unsigned tagA = (unsigned)epoch, tagB = (unsigned)(epoch >> 32);
    STAMP(1);
    v4u kf[16], vf[8][2];
    {
      const int gl = (w & 1) * 64 + lane, hw = w >> 1;
      const int off = WS_QABS + ((b * 32 + hw) * 128 + gl) * 16;
      v4u g[8];
#pragma unroll
      for (int u = 0; u < 8; ++u) g[u] = LDG(off + u * 8192);
      SCHED();
#pragma unroll
      for (int st = 0; st < 16; ++st) kf[st] = lds128(L + kt * 32768 + ((ob ^ ((st & 7) << 5)) + (st >> 3) * 256));
      const int blk = (lane >> 4) & 1, q = (lane >> 2) & 3, p = lane & 3;
      unsigned va[2][2];
#pragma unroll
      for (int t = 0; t < 2; ++t)
#pragma unroll
        for (int cc = 0; cc < 2; ++cc)
          va[t][cc] = (8 * hf + 4 * t + q) * 1024 + (((8 * w + 4 * cc + 2 * blk + (p >> 1)) ^ (4 * q + 2 * (hf ^ t) + hf)) << 4) + 8 * (p & 1);
#pragma unroll
      for (int ks = 0; ks < 8; ++ks)
#pragma unroll
        for (int cc = 0; cc < 2; ++cc) vf[ks][cc] = trfrag(L + va[0][cc] + ks * 16384, L + va[1][cc] + ks * 16384);
      SCHED();
      unsigned todo = 0;
#pragma unroll
      for (int u = 0; u < 8; ++u) if (!__all(TAGOK(g[u]))) todo |= 1u << u;
      for (unsigned spins = 0; todo != 0 && spins < SPIN_CAP; ++spins) {
        __builtin_amdgcn_s_sleep(POLL_SLEEP);
        CBAR();
#pragma unroll
        for (int u = 0; u < 8; ++u) if (todo & (1u << u)) g[u] = LDG(off + u * 8192);
#pragma unroll
        for (int u = 0; u < 8; ++u) if (todo & (1u << u)) { if (__all(TAGOK(g[u]))) todo &= ~(1u << u); }
      }
      const unsigned qb = QO + hw * 1024 + (((gl >> 1) ^ (hw << 2)) << 4) + (gl & 1) * 8;
#pragma unroll
      for (int u = 0; u < 8; ++u) *(LAS v2u*)(L + ((qb ^ (fsw((4 * u) & 15) << 4)) + u * 4096)) = v2u{g[u].y, g[u].w};
      STAMP(2);
    }
    {
      const int go = WS_OPART + (((((lane >> 3) * 4 + (w >> 1)) * 2 + b) * 64 + s) * 256) + (w & 1) * 128 + ((lane & 7) << 4);
      const v4u z = {0u, 0u, 0u, 0u};
#pragma unroll
      for (int i = 0; i < 4; ++i) STG(z, go + i * (8 * 4 * 2 * 64 * 256));
      if (lane < 32) STG(z, WS_STAT + (((b * 64 + s) * 8 + w) * 32 + lane) * 16);
    }
    LBAR();
    STAMP(3);
    CYC(0);
    f32x16 S;
#pragma unroll
    for (int i = 0; i < 16; ++i) S[i] = 0.f;
    {
      v4u qf[16];
#pragma unroll
      for (int st = 0; st < 16; ++st) qf[st] = lds128(L + QO + ((ob ^ ((st & 7) << 5)) + (st >> 3) * 256));
      SCHED();
#pragma unroll
      for (int st = 0; st < 16; ++st) S = mfma32(kf[st], qf[st], S);
    }
    CYC(1);
    CYC(2);
    float sv[8];
    if (ch == 0) {
      LAS f32x4* xw = (LAS f32x4*)(L + XCH + w * 2048 + lane * 16);
      xw[0] = f32x4{S[8], S[9], S[10], S[11]}; xw[64] = f32x4{S[12], S[13], S[14], S[15]};
#pragma unroll
      for (int e = 0; e < 8; ++e) sv[e] = S[e];
    } else {
      LAS f32x4* xw = (LAS f32x4*)(L + XCH + w * 2048 + lane * 16);
      xw[0] = f32x4{S[0], S[1], S[2], S[3]}; xw[64] = f32x4{S[4], S[5], S[6], S[7]};
#pragma unroll
      for (int e = 0; e < 8; ++e) sv[e] = S[8 + e];
    }
    LBAR();
    CYC(3);
    {
      LAS f32x4* xr = (LAS f32x4*)(L + XCH + (w ^ 4) * 2048 + lane * 16);
      const f32x4 p0 = xr[0], p1 = xr[64];
#pragma unroll
      for (int e = 0; e < 4; ++e) { sv[e] += p0[e]; sv[4 + e] += p1[e]; }
      float m = fmaxf(fmaxf(fmaxf(sv[0], sv[1]), fmaxf(sv[2], sv[3])), fmaxf(fmaxf(sv[4], sv[5]), fmaxf(sv[6], sv[7])));
      m = max32(m);
      *(LAS float*)(L + MX + r32 * 32 + w * 4) = m;
    }
    LBAR();
    CYC(4);
    float Mx;
    {
      const f32x4 m0 = *(LAS f32x4*)(L + MX + r32 * 32), m1 = *(LAS f32x4*)(L + MX + r32 * 32 + 16);
      Mx = fmaxf(fmaxf(fmaxf(m0[0], m0[1]), fmaxf(m0[2], m0[3])), fmaxf(fmaxf(m1[0], m1[1]), fmaxf(m1[2], m1[3])));
      float l = 0.f;
#pragma unroll
      for (int e = 0; e < 8; ++e) { sv[e] = __builtin_amdgcn_exp2f(sv[e] - Mx); l += sv[e]; }
#pragma unroll
      for (int i = 0; i < 2; ++i)
        *(LAS v2u*)(L + PB + r32 * 256 + (((4 * kt + 2 * ch + i) ^ fr) << 4) + hf * 8) = v2u{pk2(sv[4 * i], sv[4 * i + 1]), pk2(sv[4 * i + 2], sv[4 * i + 3])};
      l = sum32(l);
      *(LAS float*)(L + LS + r32 * 32 + w * 4) = l;
    }
    LBAR();
    CYC(5);
    STAMP(4);
    float Ls;
    f32x16 O0, O1;
#pragma unroll
    for (int i = 0; i < 16; ++i) { O0[i] = 0.f; O1[i] = 0.f; }
    {
      const unsigned pb = PB + r32 * 256 + ((hf ^ fr) << 4);
      v4u pf[8];
#pragma unroll
      for (int ks = 0; ks < 8; ++ks) pf[ks] = lds128(L + (pb ^ (ks << 5)));
      const f32x4 l0 = *(LAS f32x4*)(L + LS + r32 * 32), l1 = *(LAS f32x4*)(L + LS + r32 * 32 + 16);
      SCHED();
#pragma unroll
      for (int ks = 0; ks < 8; ++ks) {
        O0 = mfma32(vf[ks][0], pf[ks], O0);
        O1 = mfma32(vf[ks][1], pf[ks], O1);
      }
      Ls = ((l0[0] + l0[1]) + (l0[2] + l0[3])) + ((l1[0] + l1[1]) + (l1[2] + l1[3]));
    }
    CYC(6);
    {
      LAS char* ot = L + w * 4096;
      const unsigned ow = r32 * 128 + 8 * hf;
#pragma unroll
      for (int gq = 0; gq < 4; ++gq) {
        *(LAS v2u*)(ot + ow + ((gq ^ (r32 & 7)) << 4)) = v2u{pk2(O0[4 * gq], O0[4 * gq + 1]), pk2(O0[4 * gq + 2], O0[4 * gq + 3])};
        *(LAS v2u*)(ot + ow + (((4 + gq) ^ (r32 & 7)) << 4)) = v2u{pk2(O1[4 * gq], O1[4 * gq + 1]), pk2(O1[4 * gq + 2], O1[4 * gq + 3])};
      }
      CYC(7);
      STAMP(5);
      const int go = WS_OPART + (((((lane >> 3) * 4 + (w >> 1)) * 2 + b) * 64 + s) * 256) + (w & 1) * 128 + (((lane & 7) ^ ((lane >> 3) & 7)) << 4);
#pragma unroll
      for (int i = 0; i < 4; ++i) __builtin_amdgcn_raw_buffer_store_b128(lds128(ot + i * 1024 + lane * 16), rs, go + i * (8 * 4 * 2 * 64 * 256), 0, OAUX);
    }
    SCHED();
    CYC(8);
    DRAIN();
    CYC(9);
    if (lane < 32) {
      const v4u gr = {tagA, __float_as_uint(Mx), tagB, __float_as_uint(Ls)};
      STG(gr, WS_STAT + (((b * 64 + s) * 8 + w) * 32 + lane) * 16);
    }
    CYC(10);
    STAMP(6);
    if ((a & 15) < 9) {
      const int r = (a >> 4) * 9 + (a & 15), t2 = tid - 256;
      float g0 = 0.f, g1 = 0.f, bias2 = 0.f;
      if (w == 4) {
        bias2 = bflo((unsigned)b2[8 * r + (lane & 7)]);
        float d0 = 0.f, d1 = 0.f;
#pragma unroll
        for (int i = 0; i < 2; ++i) {
          const int c = lane + 64 * i;
          if (c < 72) {
            const v4u xa = gld(x + c * 8), xb = gld(x + NM + c * 8), gg = gld(gw + c * 8);
#pragma unroll
            for (int e = 0; e < 4; ++e) {
              d0 += bflo(xa[e]) * bflo(gg[e]) + bfhi(xa[e]) * bfhi(gg[e]);
              d1 += bflo(xb[e]) * bflo(gg[e]) + bfhi(xb[e]) * bfhi(gg[e]);
            }
          }
        }
        d0 = wave_sum(d0); d1 = wave_sum(d1);
        g0 = 1.f / (1.f + __expf(-d0)); g1 = 1.f / (1.f + __expf(-d1));
      }
      const int off = WS_POUT + r * 16384 + tid * 16;
      v4u ga[2];
      wait_until(t0 + T_RED);
      for (unsigned spins = 0;; ++spins) {
        CBAR();
#pragma unroll
        for (int i = 0; i < 2; ++i) ga[i] = LDG(off + i * 8192);
        if (__all(TAGOK(ga[0]) & TAGOK(ga[1])) || spins > SPIN_CAP) break;
        __builtin_amdgcn_s_sleep(POLL_SLEEP);
      }
      STAMP(9);
      float v0 = __uint_as_float(ga[0].y) + __uint_as_float(ga[1].y);
      float v1 = __uint_as_float(ga[0].w) + __uint_as_float(ga[1].w);
      v0 += dppf<0x128>(v0); v1 += dppf<0x128>(v1);
      v0 = sum32(sum16(v0)); v1 = sum32(sum16(v1));
      if (lane < 8) { *(LAS float*)(L + 65536 + (lane * 8 + w) * 4) = v0; *(LAS float*)(L + 65536 + 256 + (lane * 8 + w) * 4) = v1; }
      LBAR();
      if (t2 >= 0 && t2 < 16) {
        const f32x4 p0 = *(LAS f32x4*)(L + 65536 + t2 * 32), p1 = *(LAS f32x4*)(L + 65536 + t2 * 32 + 16);
        const float v = ((p0[0] + p0[1]) + (p0[2] + p0[3])) + ((p1[0] + p1[1]) + (p1[2] + p1[3])) + (1.f - (t2 < 8 ? g0 : g1)) * bias2;
        out[(t2 >> 3) * NM + 8 * r + (t2 & 7)] = (u16)bf1(v);
      }
      if (r == 0 && tid == 0) __hip_atomic_store((GAS u64*)(ws + WS_EPOCH), epoch, __ATOMIC_RELAXED, __HIP_MEMORY_SCOPE_AGENT);
      STAMP(10);
    }
    STAMP_FLUSH();
  }
}

extern "C" void kernel_launch(void* const* d_in, const int* in_sizes, int n_in, void* d_out, int out_size, void* d_ws, size_t ws_size,
                              hipStream_t stream) {
  fused_kernel<<<256, 512, 0, stream>>>((const u16*)d_in[0], (const u16*)d_in[1], (const u16*)d_in[2], (const u16*)d_in[3],
                                        (const u16*)d_in[4], (const u16*)d_in[5], (const u16*)d_in[7], (const u16*)d_in[8],
                                        (const u16*)d_in[9], (const u16*)d_in[10], (const u16*)d_in[11], (u16*)d_out, (char*)d_ws + WS_BASE);
}
```

```cpp
#include <hip/hip_runtime.h>
#include <hip/hip_bf16.h>
#include <stdint.h>

#define GAS __attribute__((address_space(1)))
#define LAS __attribute__((address_space(3)))
#define DI __device__ __forceinline__
typedef unsigned short u16;
typedef unsigned long long u64;
typedef unsigned v4u __attribute__((ext_vector_type(4)));
typedef unsigned v2u __attribute__((ext_vector_type(2)));
typedef float f32x2 __attribute__((ext_vector_type(2)));
typedef float f32x4 __attribute__((ext_vector_type(4)));
typedef float f32x16 __attribute__((ext_vector_type(16)));
typedef short bf16x8 __attribute__((ext_vector_type(8)));
typedef short s16x4 __attribute__((ext_vector_type(4)));
typedef __bf16 bf2_t __attribute__((ext_vector_type(2)));

constexpr int NM = 576, NH = 32, ND = 128, NC = 512, NSK = 8192, NF = 4608;
constexpr int WS_EPOCH = 0;
constexpr int WS_HX = 4096;
constexpr int WS_QX = WS_HX + 4 * 73728;
constexpr int WS_CX = WS_QX + 262144;
constexpr int WS_QABS = WS_CX + 262144;
constexpr int WS_STAT = WS_QABS + 131072;
constexpr int WS_POUT = WS_STAT + 524288;
constexpr int WS_OPART = WS_POUT + 72 * 128 * 128;
constexpr int WS_STAMP = WS_OPART + 4194304;
constexpr int WS_BYTES = WS_STAMP + 2 * 256 * 256;
constexpr int AUX_SC1 = 16;
constexpr unsigned SPIN_CAP = 1u << 17;

#define CKV_N1 0
#define CKV_WIN 2
#define CKV_WIN2 16
#define PRETOUCH 0
#define XPOL ""
#define STAT_SW 1
#define STAT_SLEEP 2
#define OAUX 16
#define OPRE 2
#define WS_BASE 0
#define BULK_WIN 32
#define PPRE 0
#define HO0_SLEEP 3
#define T_CKV 150
#define POLL_SLEEP 2
#define T_STATS 850
#define T_HEADB 470
#define T_RED 800

#define STAMP(i) do {} while (0)
#define STAMPW(wv, i) do {} while (0)
#define STAMP_FLUSH() do {} while (0)
#define CYC(i) do {} while (0)

DI unsigned fsw(unsigned r) { return ((r & 3u) << 2) | ((((r >> 3) ^ (r >> 2)) & 1u) << 1) | ((r >> 3) & 1u); }
DI float bflo(unsigned v) { return __uint_as_float(v << 16); }
DI float bfhi(unsigned v) { return __uint_as_float(v & 0xffff0000u); }
DI unsigned pk2(float a, float b) { f32x2 f = {a, b}; bf2_t r = __builtin_convertvector(f, bf2_t); return __builtin_bit_cast(unsigned, r); }
DI unsigned bf1(float a) { return pk2(a, 0.f) & 0xffffu; }
DI f32x4 mfma16(v4u a, v4u b, f32x4 c) { return __builtin_amdgcn_mfma_f32_16x16x32_bf16(__builtin_bit_cast(bf16x8, a), __builtin_bit_cast(bf16x8, b), c, 0, 0, 0); }
DI f32x16 mfma32(v4u a, v4u b, f32x16 c) { return __builtin_amdgcn_mfma_f32_32x32x16_bf16(__builtin_bit_cast(bf16x8, a), __builtin_bit_cast(bf16x8, b), c, 0, 0, 0); }
DI v2u trread(LAS char* p) { s16x4 r = __builtin_amdgcn_ds_read_tr16_b64_v4i16((LAS s16x4*)p); return __builtin_bit_cast(v2u, r); }
DI v4u trfrag(LAS char* p0, LAS char* p1) { v2u a = trread(p0), b = trread(p1); return v4u{a.x, a.y, b.x, b.y}; }
DI v4u lds128(LAS char* p) { return *(LAS v4u*)p; }
DI void dma16(const u16* g, LAS char* l) { __builtin_amdgcn_global_load_lds((const GAS unsigned*)g, (LAS unsigned*)l, 16, 0, 2); }
DI v4u gld(const u16* p) { return *(const GAS v4u*)p; }
DI v4u gldnt(const u16* p) { return __builtin_nontemporal_load((const GAS v4u*)p); }
DI v2u gldnt8(const u16* p) { return __builtin_nontemporal_load((const GAS v2u*)p); }
template <int CTRL> DI float dppf(float x) { return __builtin_bit_cast(float, __builtin_amdgcn_mov_dpp(__builtin_bit_cast(int, x), CTRL, 0xf, 0xf, true)); }
template <int CTRL> DI unsigned dppu(unsigned x) { return (unsigned)__builtin_amdgcn_mov_dpp((int)x, CTRL, 0xf, 0xf, true); }
DI float sum32(float x) { auto t = __builtin_amdgcn_permlane32_swap(__float_as_uint(x), __float_as_uint(x), false, false); return __uint_as_float(t[0]) + __uint_as_float(t[1]); }
DI float max32(float x) { auto t = __builtin_amdgcn_permlane32_swap(__float_as_uint(x), __float_as_uint(x), false, false); return fmaxf(__uint_as_float(t[0]), __uint_as_float(t[1])); }
DI float sum16(float x) { auto t = __builtin_amdgcn_permlane16_swap(__float_as_uint(x), __float_as_uint(x), false, false); return __uint_as_float(t[0]) + __uint_as_float(t[1]); }
DI float max16(float x) { auto t = __builtin_amdgcn_permlane16_swap(__float_as_uint(x), __float_as_uint(x), false, false); return fmaxf(__uint_as_float(t[0]), __uint_as_float(t[1])); }
DI float wave_sum(float x) { x += dppf<0xB1>(x); x += dppf<0x4E>(x); x += dppf<0x141>(x); x += dppf<0x128>(x); x = sum16(x); return sum32(x); }
DI float wave_max(float x) { x = fmaxf(x, dppf<0xB1>(x)); x = fmaxf(x, dppf<0x4E>(x)); x = fmaxf(x, dppf<0x141>(x)); x = fmaxf(x, dppf<0x128>(x)); x = max16(x); return max32(x); }
DI void wait_until(u64 t) { while (__builtin_amdgcn_s_memrealtime() < t) __builtin_amdgcn_s_sleep(1); }
#define DRAIN() asm volatile("s_waitcnt vmcnt(0)" ::: "memory")
#define CBAR() asm volatile("" ::: "memory")
#define SCHED() __builtin_amdgcn_sched_barrier(0)
#define LBAR() asm volatile("s_waitcnt lgkmcnt(0)\n\ts_barrier" ::: "memory")
#define DMA16(gp, ldsoff) do { unsigned keep_; asm volatile("s_mov_b32 %0, m0\n\ts_mov_b32 m0, %2\n\ts_nop 0\n\tglobal_load_lds_dwordx4 %1, off nt\n\ts_mov_b32 m0, %0" : "=&s"(keep_) : "v"(gp), "s"((unsigned)(size_t)L + (unsigned)(ldsoff)) : "memory"); } while (0)
#define TOUCH(dst, off) asm volatile("buffer_load_dword %0, %1, %2, 0 offen sc1" : "=&v"(dst) : "v"(off), "s"(rs) : "memory")
#define DMA16X(gp, ldsoff) do { unsigned keep_; asm volatile("s_mov_b32 %0, m0\n\ts_mov_b32 m0, %2\n\ts_nop 0\n\tglobal_load_lds_dwordx4 %1, off" XPOL "\n\ts_mov_b32 m0, %0" : "=&s"(keep_) : "v"(gp), "s"((unsigned)(size_t)L + (unsigned)(ldsoff)) : "memory"); } while (0)
#define DMA16W(gp, ldsoff) do { DMA16(gp, ldsoff); if (BULK_WIN < 32) asm volatile("s_waitcnt vmcnt(%0)" :: "i"(BULK_WIN - 1) : "memory"); } while (0)
#define LDG(off) __builtin_amdgcn_raw_buffer_load_b128(rs, (off), 0, AUX_SC1)
#define STG(v, off) __builtin_amdgcn_raw_buffer_store_b128((v), rs, (off), 0, AUX_SC1)
#define TAGOK(g) (((g).x == tagA) & ((g).z == tagB))

DI int tile_lane_off(int lane, int ld) { const int r8 = lane >> 3; return (8 * (r8 >> 2) + (r8 & 3)) * ld + 8 * ((lane & 7) ^ ((r8 >> 1) << 1)); }
DI int tile_tr_off(int lane, int ct) { const int r8 = 4 * ((lane >> 4) & 1) + ((lane >> 2) & 3); return (lane >> 5) * 1024 + r8 * 128 + ((ct ^ (r8 >> 1)) * 32) + (lane & 3) * 8; }
DI void put_hilo(LAS char* base, int stride, int d, float v0, float v1) {
  const unsigned h = pk2(v0, v1);
  const unsigned l = pk2(v0 - bflo(h), v1 - bfhi(h));
  *(LAS u16*)(base + d * 2) = (u16)h;
  *(LAS u16*)(base + stride + d * 2) = (u16)(h >> 16);
  *(LAS u16*)(base + 2 * stride + d * 2) = (u16)l;
  *(LAS u16*)(base + 3 * stride + d * 2) = (u16)(l >> 16);
}

__global__ void __launch_bounds__(512, 2)
fused_kernel(const u16* __restrict__ x, const u16* __restrict__ ckv, const u16* __restrict__ wq, const u16* __restrict__ wuk,
             const u16* __restrict__ wuv, const u16* __restrict__ wo, const u16* __restrict__ w1, const u16* __restrict__ b1,
             const u16* __restrict__ w2, const u16* __restrict__ b2, const u16* __restrict__ gw, u16* __restrict__ out, char* ws) {
  __shared__ __attribute__((aligned(16))) char smem[163840];
  LAS char* L = (LAS char*)smem;
  const int tid = threadIdx.x, lane = tid & 63, w = __builtin_amdgcn_readfirstlane(tid >> 6);
  const int bid = blockIdx.x, xcd = bid & 7, kk = bid >> 3;
  const u64 t0 = __builtin_amdgcn_s_memrealtime();
  const __amdgpu_buffer_rsrc_t rs = __builtin_amdgcn_make_buffer_rsrc((void*)ws, 0, WS_BYTES, 0x00020000);
#define LOAD_EPOCH() (__hip_atomic_load((GAS u64*)(ws + WS_EPOCH), __ATOMIC_RELAXED, __HIP_MEMORY_SCOPE_AGENT) + 1ull)
  STAMP(0);
  const int g4 = lane >> 4, col = lane & 15;

  if (kk < 16) {
    const int h = xcd * 4 + (kk >> 2), j = kk & 3;
    constexpr int WQ = 0, WUK = 40960, W1 = 69632, W2 = W1 + 40960, W2X = W2 + 36864, WUV = 0, WO = 32768;
    constexpr int RED = W2X + 9216, CTXA = RED + 4096, QA = CTXA + 1024, PRIV = RED, HR = QA + 512;
    const int ks0 = j < 2 ? 5 * j : 4 * j + 2;
    u64 epoch;
    asm volatile("global_load_dwordx2 %0, %1, off sc1" : "=&v"(epoch) : "v"(ws + WS_EPOCH) : "memory");
    constexpr int XG = W2X;
    if (w < 3) { const int c = 64 * w + lane; DMA16X(x + 8 * (c < 144 ? c : 0), XG + w * 1024); }
    else if (w < 5) { const int c = 64 * (w - 3) + lane; DMA16X(gw + 8 * (c < 72 ? c : 0), XG + w * 1024); }
    {
      const u16* src = wq + (size_t)(32 * ks0) * 4096 + h * ND + tile_lane_off(lane, 4096) + (16 * (w & 1) + 4 * (w >> 2)) * 4096 + 64 * ((w >> 1) & 1);
#pragma unroll
      for (int i = 0; i < 5; ++i)
        if (i < 4 || j < 2) DMA16(src + (size_t)i * 32 * 4096, WQ + (i * 8 + w) * 1024);
#pragma unroll
      for (int i = 0; i < 4; ++i) {
        const int row = 16 * w + 4 * i + g4;
        DMA16(wuk + (size_t)(128 * j + row) * 4096 + h * ND + 8 * (col ^ fsw(4 * i + g4)), WUK + (4 * w + i) * 1024);
      }
    }
    asm volatile("s_waitcnt vmcnt(4)\n\ts_barrier" : "+v"(epoch) :: "memory");
    v4u xa[5];
#pragma unroll
    for (int i = 0; i < 5; ++i) xa[i] = lds128(L + XG + (col & 1) * 1152 + 64 * (ks0 + ((i < 4 || j < 2) ? i : 0)) + 16 * g4);
    epoch += 1ull;
    if ((unsigned)epoch == 0u) epoch += 1ull;
    const unsigned tagA = (unsigned)epoch, tagB = (unsigned)(epoch >> 32);
    STAMP(1);
    {
      f32x4 acc = {0.f, 0.f, 0.f, 0.f};
      LAS char* p = L + WQ + (w >> 2) * 2048 + tile_tr_off(lane, w & 3);
      v4u bq[5];
#pragma unroll
      for (int i = 0; i < 4; ++i) bq[i] = trfrag(p + i * 8192, p + i * 8192 + 4096);
      if (j < 2) bq[4] = trfrag(p + 4 * 8192, p + 4 * 8192 + 4096);
#pragma unroll
      for (int i = 0; i < 4; ++i) acc = mfma16(xa[i], bq[i], acc);
      if (j < 2) acc = mfma16(xa[4], bq[4], acc);
      if (g4 == 0) { const v4u gr = {tagA, __float_as_uint(acc[0]), tagB, __float_as_uint(acc[1])}; STG(gr, WS_QX + ((h * 4 + j) * 128 + 16 * w + col) * 16); }
    }
    STAMP(2);
    float g0, g1;
    {
      const int gc = lane + 64 < 72 ? lane + 64 : 0;
      const v4u gq[6] = {lds128(L + XG + lane * 16), lds128(L + XG + 1152 + lane * 16), lds128(L + XG + 3072 + lane * 16),
                         lds128(L + XG + gc * 16), lds128(L + XG + 1152 + gc * 16), lds128(L + XG + 3072 + gc * 16)};
      float d0 = 0.f, d1 = 0.f, e0 = 0.f, e1 = 0.f;
#pragma unroll
      for (int e = 0; e < 4; ++e) {
        d0 += bflo(gq[0][e]) * bflo(gq[2][e]) + bfhi(gq[0][e]) * bfhi(gq[2][e]);
        d1 += bflo(gq[1][e]) * bflo(gq[2][e]) + bfhi(gq[1][e]) * bfhi(gq[2][e]);
        e0 += bflo(gq[3][e]) * bflo(gq[5][e]) + bfhi(gq[3][e]) * bfhi(gq[5][e]);
        e1 += bflo(gq[4][e]) * bflo(gq[5][e]) + bfhi(gq[4][e]) * bfhi(gq[5][e]);
      }
      if (lane < 8) { d0 += e0; d1 += e1; }
      d0 = wave_sum(d0); d1 = wave_sum(d1);
      g0 = 1.f / (1.f + __expf(-d0)); g1 = 1.f / (1.f + __expf(-d1));
      asm volatile("" : "+v"(g0), "+v"(g1));
    }
    DRAIN();
    __builtin_amdgcn_s_sleep(HO0_SLEEP);
    LAS char* pv = L + PRIV + w * 256;
    {
      v4u ga;
      const int off = WS_QX + ((h * 4 + g4) * 128 + 16 * w + col) * 16;
      for (unsigned spins = 0;; ++spins) {
        CBAR();
        ga = LDG(off);
        if (__all(TAGOK(ga)) || spins > SPIN_CAP) break;
        __builtin_amdgcn_s_sleep(1);
      }
      STAMP(3);
      v4u uk[4];
#pragma unroll
      for (int ks = 0; ks < 4; ++ks) uk[ks] = lds128(L + WUK + (16 * w + col) * 256 + (((4 * ks + g4) ^ fsw(col)) << 4));
      const float q0 = sum32(sum16(__uint_as_float(ga.y))), q1 = sum32(sum16(__uint_as_float(ga.w)));
      const float qv = (g4 & 1) ? q1 : q0;
      const unsigned hi = bf1(qv);
      *(LAS u16*)(L + QA + g4 * 256 + (16 * w + col) * 2) = (u16)((g4 & 2) ? bf1(qv - bflo(hi)) : hi);
      LBAR();
      f32x4 a = {0.f, 0.f, 0.f, 0.f};
#pragma unroll
      for (int ks = 0; ks < 4; ++ks) a = mfma16(lds128(L + QA + (col & 3) * 256 + ks * 64 + g4 * 16), uk[ks], a);
      const float v = ((g4 & 1) ? (a[1] + a[3]) : (a[0] + a[2])) * (0.08838834764831845f * 1.4426950408889634f);
      const unsigned bits = bf1(v);
      const unsigned n1 = dppu<0x55>(bits), n2 = dppu<0xAA>(bits), n3 = dppu<0xFF>(bits);
      if ((lane & 3) == 0 && g4 < 2) {
        const v4u gr = {tagA, bits | (n1 << 16), tagB, n2 | (n3 << 16)};
        STG(gr, WS_QABS + ((g4 * 32 + h) * 128 + 32 * j + 4 * w + (col >> 2)) * 16);
      }
    }
    STAMP(4);
    const int pidx = h * 4 + j;
    const bool dbl = kk >= 14;
    const int u0 = (xcd & 1) ? (dbl ? 12 + 4 * (kk - 14) : kk < 12 ? kk : kk + 2) : (dbl ? 10 + 4 * (kk - 14) : kk < 10 ? kk : kk < 12 ? kk + 2 : kk + 4);
    const int r2 = dbl ? 4480 + 8 * (xcd * 2 + kk - 14) : 40 * (xcd * 14 + kk);
    const int nk2 = dbl ? 8 : 40;
    unsigned b1v[3];
    {
      const int ug = xcd * 18 + u0, uq = ug & 3, f0 = 128 * (ug >> 2);
      const int kq0 = uq < 2 ? 5 * uq : 4 * uq + 2, kq1 = 14;
      v4u xm[2][5];
#pragma unroll
      for (int i = 0; i < 5; ++i) {
        const int ki = (i < 4 || uq < 2) ? i : 0;
        xm[0][i] = lds128(L + XG + (col & 1) * 1152 + 64 * (kq0 + ki) + 16 * g4);
        xm[1][i] = lds128(L + XG + (col & 1) * 1152 + 64 * (kq1 + ki) + 16 * g4);
      }
      LBAR();
      if (T_HEADB > 0) wait_until(t0 + T_HEADB);
      {
        const u16* src = wuv + (size_t)(128 * j) * 4096 + h * ND + tile_lane_off(lane, 4096) + (16 * (w & 1) + 4 * (w >> 2)) * 4096 + 64 * ((w >> 1) & 1);
#pragma unroll
        for (int i = 0; i < 4; ++i) DMA16W(src + (size_t)i * 32 * 4096, WUV + (i * 8 + w) * 1024);
        const u16* so = wo + (size_t)(h * ND + 32 * j) * NM + tile_lane_off(lane, NM) + (16 * (w & 1) + 4 * ((w >> 1) & 1)) * NM + 64 * (w >> 2);
#pragma unroll
        for (int i = 0; i < 5; ++i)
          if (i < 4 || w < 4) DMA16W(so + 128 * i, WO + (i * 8 + w) * 1024);
      }
      {
        const int r8 = lane >> 3;
        const u16* s3 = w2 + (size_t)(r2 + nk2 - 8 + r8) * NM + 8 * ((lane & 7) ^ ((r8 >> 1) << 1));
        DMA16W(s3 + 64 * w, W2X + w * 1024);
        if (w == 0) DMA16W(s3 + 512, W2X + 8192);
      }
      if (!dbl) {
        const u16* s2 = w2 + (size_t)r2 * NM + tile_lane_off(lane, NM) + (16 * (w & 1) + 4 * ((w >> 1) & 1)) * NM + 64 * (w >> 2);
#pragma unroll
        for (int i = 0; i < 5; ++i)
          if (i < 4 || w < 4) DMA16W(s2 + 128 * i, W2 + (i * 8 + w) * 1024);
      }
      {
        const int fb = r2 + col;
        asm volatile("global_load_ushort %0, %3, off\n\tglobal_load_ushort %1, %4, off\n\tglobal_load_ushort %2, %5, off"
                     : "=&v"(b1v[0]), "=&v"(b1v[1]), "=&v"(b1v[2])
                     : "v"(b1 + (col < nk2 ? fb : r2)), "v"(b1 + (16 + col < nk2 ? fb + 16 : r2)), "v"(b1 + (32 + col < nk2 ? fb + 32 : r2)) : "memory");
      }
      {
        const u16* s1 = w1 + f0 + tile_lane_off(lane, NF) + (size_t)(16 * (w & 1) + 4 * (w >> 2)) * NF + 64 * ((w >> 1) & 1);
#pragma unroll
        for (int i = 0; i < 5; ++i)
          if (i < 4 || uq < 2) DMA16W(s1 + (size_t)(kq0 + i) * 32 * NF, W1 + (i * 8 + w) * 1024);
        if (dbl) {
#pragma unroll
          for (int i = 0; i < 4; ++i) DMA16W(s1 + (size_t)(kq1 + i) * 32 * NF, W2 + (i * 8 + w) * 1024);
        }
      }
      asm volatile("s_waitcnt vmcnt(0)\n\ts_barrier" : "+v"(b1v[0]), "+v"(b1v[1]), "+v"(b1v[2]) :: "memory");
      STAMP(10);
#pragma unroll
      for (int un = 0; un < 2; ++un) {
        if (un == 0 || dbl) {
          LAS char* p = L + (un == 0 ? W1 : W2) + (w >> 2) * 2048 + tile_tr_off(lane, w & 3);
          f32x4 acc = {0.f, 0.f, 0.f, 0.f};
#pragma unroll
          for (int i = 0; i < 4; ++i) acc = mfma16(xm[un][i], trfrag(p + i * 8192, p + i * 8192 + 4096), acc);
          if (uq < 2) acc = mfma16(xm[un][4], trfrag(p + 4 * 8192, p + 4 * 8192 + 4096), acc);
          if (g4 == 0) {
            const v4u gr = {tagA, __float_as_uint(acc[0]), tagB, __float_as_uint(acc[1])};
            STG(gr, WS_HX + ((uq + un) * NF + f0 + 16 * w + col) * 16);
          }
        }
      }
      STAMP(11);
    }
    v4u uv[4], wf[5];
    {
      LAS char* p = L + WUV + (w >> 2) * 2048 + tile_tr_off(lane, w & 3);
#pragma unroll
      for (int i = 0; i < 4; ++i) uv[i] = trfrag(p + i * 8192, p + i * 8192 + 4096);
      LAS char* po = L + WO + (w >> 2) * 4096 + tile_tr_off(lane, w & 3);
#pragma unroll
      for (int i = 0; i < 5; ++i) wf[i] = (i < 4 || w < 4) ? trfrag(po + i * 8192, po + i * 8192 + 2048) : v4u{0u, 0u, 0u, 0u};
    }
    v4u w2m[5], w2x[5];
    {
      const int q = (lane >> 2) & 3;
      LAS char* p = L + W2X + (w >> 2) * 1024 + q * 128 + (lane & 3) * 8;
      const int o0 = (((w & 3) ^ (q >> 1)) * 32), o1 = 512 + (((w & 3) ^ (2 + (q >> 1))) * 32);
      LAS char* pm = L + W2 + (w >> 2) * 4096 + tile_tr_off(lane, w & 3);
#pragma unroll
      for (int i = 0; i < 5; ++i) {
        const int ii = (i < 4 || w < 4) ? i : 0;
        w2x[i] = trfrag(p + ii * 2048 + o0, p + ii * 2048 + o1);
        w2m[i] = trfrag(pm + ii * 8192, pm + ii * 8192 + 2048);
        if (dbl) w2m[i] = v4u{0u, 0u, 0u, 0u};
      }
    }
    STAMP(5);
    const int b = w >> 2, q4 = w & 3;
    float wgt;
    v4u hg = {0u, 0u, 0u, 0u}, cxw = {0u, 0u, 0u, 0u};
    const int ht = w - 2, hf_ = 16 * ht + col;
    const int ho = WS_HX + (g4 * NF + r2 + (hf_ < nk2 ? hf_ : 0)) * 16;
    constexpr int HA = QA;
    {
      const int off = WS_STAT + ((h * 2 + b) * 64 + 16 * q4 + ((lane >> 1) & 15)) * 16;
      const int ooff = WS_OPART + ((((h * 4 + j) * 2 + b) * 64 + 16 * q4) * 256) + lane * 16;
      v4u g, o[4];
      bool ok = lane >= 32;
      unsigned done = 0;
      if (w < 2) { cxw = LDG(WS_CX + ((h * 4 + j) * 128 + w * 64 + lane) * 16); asm volatile("" :: "v"(cxw)); }
      wait_until(t0 + T_STATS);
      for (unsigned spins = 0;; ++spins) {
        CBAR();
        if (!ok) { g = LDG(off); ok = TAGOK(g); }
        const unsigned m = (unsigned)__builtin_amdgcn_ballot_w64(ok) | (spins > SPIN_CAP ? 0xffffffffu : 0u);
        __builtin_amdgcn_fence(__ATOMIC_ACQUIRE, "wavefront");
#pragma unroll
        for (int i = 0; i < 4; ++i)
          if (!(done & (1u << i)) && ((m >> (8 * i)) & 0xffu) == 0xffu) { o[i] = LDG(ooff + i * 1024); done |= 1u << i; }
        if (done == 15u) break;
        __builtin_amdgcn_s_sleep(STAT_SLEEP);
      }
      if (ht >= 0 && ht < 3) hg = LDG(ho);
      STAMPW(0, 6);
      const float ms = lane < 32 ? __uint_as_float(g.y) : -INFINITY, ls = (lane < 32 && !(lane & 1)) ? __uint_as_float(g.w) : 0.f;
      float Mw = fmaxf(ms, dppf<0x4E>(ms));
      Mw = fmaxf(Mw, dppf<0x141>(Mw)); Mw = fmaxf(Mw, dppf<0x128>(Mw)); Mw = max32(max16(Mw));
      wgt = __builtin_amdgcn_exp2f(ms - Mw);
      float ww[4];
#pragma unroll
      for (int i = 0; i < 4; ++i) ww[i] = __uint_as_float((unsigned)__builtin_amdgcn_ds_bpermute((4 * i + g4) * 8, (int)__float_as_uint(wgt)));
      float Lw = wgt * ls;
      Lw += dppf<0xB1>(Lw); Lw += dppf<0x4E>(Lw); Lw += dppf<0x141>(Lw); Lw += dppf<0x128>(Lw); Lw = sum32(sum16(Lw));
      if (lane == 0) *(LAS f32x2*)(L + HR + w * 8) = f32x2{Mw, Lw};
      asm volatile("" : "+v"(ww[0]), "+v"(ww[1]), "+v"(ww[2]), "+v"(ww[3]));
      SCHED();
      float acc[8] = {0.f, 0.f, 0.f, 0.f, 0.f, 0.f, 0.f, 0.f};
#pragma unroll
      for (int i = 0; i < 4; ++i) {
        acc[0] += ww[i] * bflo(o[i].x); acc[1] += ww[i] * bfhi(o[i].x);
        acc[2] += ww[i] * bflo(o[i].y); acc[3] += ww[i] * bfhi(o[i].y);
        acc[4] += ww[i] * bflo(o[i].z); acc[5] += ww[i] * bfhi(o[i].z);
        acc[6] += ww[i] * bflo(o[i].w); acc[7] += ww[i] * bfhi(o[i].w);
      }
      float r4[4];
#pragma unroll
      for (int e = 0; e < 4; ++e) {
        auto t = __builtin_amdgcn_permlane32_swap(__float_as_uint(acc[e]), __float_as_uint(acc[4 + e]), false, false);
        r4[e] = sum16(__uint_as_float(t[0]) + __uint_as_float(t[1]));
      }
      if ((g4 & 1) == 0) *(LAS f32x4*)(L + RED + w * 512 + col * 32 + (lane >> 5) * 16) = f32x4{r4[0], r4[1], r4[2], r4[3]};
    }
    LBAR();
    if (tid < 128) {
      LAS float* r = (LAS float*)(L + RED) + tid;
      const f32x4 s0 = *(LAS f32x4*)(L + HR), s1 = *(LAS f32x4*)(L + HR + 16), s2 = *(LAS f32x4*)(L + HR + 32), s3 = *(LAS f32x4*)(L + HR + 48);
      const float Ma = fmaxf(fmaxf(s0[0], s0[2]), fmaxf(s1[0], s1[2])), Mb = fmaxf(fmaxf(s2[0], s2[2]), fmaxf(s3[0], s3[2]));
      const float a0 = __builtin_amdgcn_exp2f(s0[0] - Ma), a1 = __builtin_amdgcn_exp2f(s0[2] - Ma), a2 = __builtin_amdgcn_exp2f(s1[0] - Ma), a3 = __builtin_amdgcn_exp2f(s1[2] - Ma);
      const float c0 = __builtin_amdgcn_exp2f(s2[0] - Mb), c1 = __builtin_amdgcn_exp2f(s2[2] - Mb), c2 = __builtin_amdgcn_exp2f(s3[0] - Mb), c3 = __builtin_amdgcn_exp2f(s3[2] - Mb);
      const float ia = __builtin_amdgcn_rcpf((a0 * s0[1] + a1 * s0[3]) + (a2 * s1[1] + a3 * s1[3]));
      const float ib = __builtin_amdgcn_rcpf((c0 * s2[1] + c1 * s2[3]) + (c2 * s3[1] + c3 * s3[3]));
      put_hilo(L + CTXA, 256, tid, ((a0 * r[0] + a1 * r[128]) + (a2 * r[256] + a3 * r[384])) * ia, ((c0 * r[512] + c1 * r[640]) + (c2 * r[768] + c3 * r[896])) * ib);
    } else if (w < 5) {
      for (unsigned spins = 0; !__all(TAGOK(hg)) && spins < SPIN_CAP; ++spins) {
        __builtin_amdgcn_s_sleep(POLL_SLEEP);
        CBAR();
        hg = LDG(ho);
      }
      const float bb = bflo(ht == 0 ? b1v[0] : ht == 1 ? b1v[1] : b1v[2]);
      const float p0 = fmaxf(sum32(sum16(__uint_as_float(hg.y))) + bb, 0.f), p1 = fmaxf(sum32(sum16(__uint_as_float(hg.w))) + bb, 0.f);
      const float hv = hf_ < nk2 ? ((g4 & 1) ? p1 * p1 : p0 * p0) : 0.f;
      const unsigned hi = bf1(hv);
      const int kpos = dbl ? 32 - 16 * ht + col : hf_;
      *(LAS u16*)(L + HA + g4 * 128 + kpos * 2) = (u16)((g4 & 2) ? bf1(hv - bflo(hi)) : hi);
    } else if (w == 5) {
      *(LAS u16*)(L + HA + g4 * 128 + (48 + col) * 2) = 0;
    }
    LBAR();
    {
      f32x4 acc = {0.f, 0.f, 0.f, 0.f};
#pragma unroll
      for (int i = 0; i < 4; ++i) acc = mfma16(lds128(L + CTXA + (col & 3) * 256 + i * 64 + g4 * 16), uv[i], acc);
      if (g4 == 0) {
        const v4u gr = {tagA, __float_as_uint(acc[0] + acc[2]), tagB, __float_as_uint(acc[1] + acc[3])};
        STG(gr, WS_CX + (((h * 4 + (w >> 1)) * 4 + j) * 32 + 16 * (w & 1) + col) * 16);
      }
    }
    STAMP(7);
    f32x2 mpa, mpb;
    {
      const v4u ha0 = lds128(L + HA + (col & 3) * 128 + g4 * 16), hax = lds128(L + HA + (col & 3) * 128 + 64 + g4 * 16);
      f32x4 acc[5];
#pragma unroll
      for (int i = 0; i < 5; ++i) acc[i] = mfma16(ha0, w2m[i], mfma16(hax, w2x[i], f32x4{0.f, 0.f, 0.f, 0.f}));
      const f32x4 a = g4 == 0 ? acc[0] : g4 == 1 ? acc[1] : g4 == 2 ? acc[2] : acc[3];
      mpa = f32x2{(1.f - g0) * (a[0] + a[2]), (1.f - g1) * (a[1] + a[3])};
      mpb = f32x2{(1.f - g0) * (acc[4][0] + acc[4][2]), (1.f - g1) * (acc[4][1] + acc[4][3])};
    }
    {
      v4u ga, gb;
      const int off = WS_CX + ((h * 4 + j) * 128 + lane) * 16;
      for (unsigned spins = 0;; ++spins) {
        CBAR();
        ga = LDG(off); gb = LDG(off + 1024);
        if (__all(TAGOK(ga) & TAGOK(gb)) || spins > SPIN_CAP) break;
        __builtin_amdgcn_s_sleep(1);
      }
      STAMP(8);
      const float v0 = sum32(__uint_as_float(ga.y) + __uint_as_float(gb.y)), v1 = sum32(__uint_as_float(ga.w) + __uint_as_float(gb.w));
      if (lane < 32) put_hilo(pv, 64, lane, v0, v1);
      const v4u ca = lds128(pv + (col & 3) * 64 + g4 * 16);
      f32x4 acc[5];
#pragma unroll
      for (int i = 0; i < 5; ++i) acc[i] = mfma16(ca, wf[i], f32x4{0.f, 0.f, 0.f, 0.f});
      const f32x4 a = g4 == 0 ? acc[0] : g4 == 1 ? acc[1] : g4 == 2 ? acc[2] : acc[3];
      {
        const int ct = w + 8 * g4;
        const f32x2 mp = mpa;
        const v4u gr = {tagA, __float_as_uint(g0 * (a[0] + a[2]) + mp.x), tagB, __float_as_uint(g1 * (a[1] + a[3]) + mp.y)};
        STG(gr, WS_POUT + ((2 * ct + (col >> 3)) * 128 + pidx) * 128 + (col & 7) * 16);
      }
      if (w < 4 && g4 == 0) {
        const int ct = w + 32;
        const f32x2 mp = mpb;
        const v4u gr = {tagA, __float_as_uint(g0 * (acc[4][0] + acc[4][2]) + mp.x), tagB, __float_as_uint(g1 * (acc[4][1] + acc[4][3]) + mp.y)};
        STG(gr, WS_POUT + ((2 * ct + (col >> 3)) * 128 + pidx) * 128 + (col & 7) * 16);
      }
    }
    STAMP(9);
    STAMP_FLUSH();
    __builtin_amdgcn_endpgm();
  } else {
    const int a = xcd * 16 + (kk - 16), b = a >> 6, s = a & 63;
    constexpr int QO = 131072, XCH = 32768, PB = XCH + 16384, MX = XCH + 24576, LS = XCH + 25600;
    u64 epoch;
    asm volatile("global_load_dwordx2 %0, %1, off sc1" : "=&v"(epoch) : "v"(ws + WS_EPOCH) : "memory");
    {
      const u16* src = ckv + ((size_t)b * NSK + 128 * s + 16 * w) * NC;
#pragma unroll
      for (int i = 0; i < 16; ++i) {
        if (i == CKV_N1) wait_until(t0 + T_CKV);
        DMA16(src + i * NC + ((lane ^ fsw(i)) * 8), (16 * w + i) * 1024);
        if (i < CKV_N1 && i >= CKV_WIN - 1) asm volatile("s_waitcnt vmcnt(%0)" :: "i"(CKV_WIN - 1) : "memory");
        if (i >= CKV_N1 && i < 15 && CKV_WIN2 < 16) asm volatile("s_waitcnt vmcnt(%0)" :: "i"(CKV_WIN2 - 1) : "memory");
      }
    }
    const int r32 = lane & 31, hf = lane >> 5, kt = w & 3, ch = w >> 2;
    const unsigned fr = fsw(r32 & 15);
    const unsigned ob = r32 * 1024 + ch * 512 + ((hf ^ fr) << 4);
    asm volatile("s_waitcnt vmcnt(0)\n\ts_barrier" : "+v"(epoch) :: "memory");
    epoch += 1ull;
    if ((unsigned)epoch == 0u) epoch += 1ull;
    const unsigned tagA = (unsigned)epoch, tagB = (unsigned)(epoch >> 32);
    STAMP(1);
    v4u kf[16], vf[8][2];
    {
      const int gl = (w & 1) * 64 + lane, hw = w >> 1;
      const int off = WS_QABS + ((b * 32 + hw) * 128 + gl) * 16;
      v4u g[8];
#pragma unroll
      for (int u = 0; u < 8; ++u) g[u] = LDG(off + u * 8192);
      SCHED();
#pragma unroll
      for (int st = 0; st < 16; ++st) kf[st] = lds128(L + kt * 32768 + ((ob ^ ((st & 7) << 5)) + (st >> 3) * 256));
      const int blk = (lane >> 4) & 1, q = (lane >> 2) & 3, p = lane & 3;
      unsigned va[2][2];
#pragma unroll
      for (int t = 0; t < 2; ++t)
#pragma unroll
        for (int cc = 0; cc < 2; ++cc)
          va[t][cc] = (8 * hf + 4 * t + q) * 1024 + (((8 * w + 4 * cc + 2 * blk + (p >> 1)) ^ (4 * q + 2 * (hf ^ t) + hf)) << 4) + 8 * (p & 1);
#pragma unroll
      for (int ks = 0; ks < 8; ++ks)
#pragma unroll
        for (int cc = 0; cc < 2; ++cc) vf[ks][cc] = trfrag(L + va[0][cc] + ks * 16384, L + va[1][cc] + ks * 16384);
      SCHED();
      unsigned todo = 0;
#pragma unroll
      for (int u = 0; u < 8; ++u) if (!__all(TAGOK(g[u]))) todo |= 1u << u;
      for (unsigned spins = 0; todo != 0 && spins < SPIN_CAP; ++spins) {
        __builtin_amdgcn_s_sleep(POLL_SLEEP);
        CBAR();
#pragma unroll
        for (int u = 0; u < 8; ++u) if (todo & (1u << u)) g[u] = LDG(off + u * 8192);
#pragma unroll
        for (int u = 0; u < 8; ++u) if (todo & (1u << u)) { if (__all(TAGOK(g[u]))) todo &= ~(1u << u); }
      }
      const unsigned qb = QO + hw * 1024 + (((gl >> 1) ^ (hw << 2)) << 4) + (gl & 1) * 8;
#pragma unroll
      for (int u = 0; u < 8; ++u) *(LAS v2u*)(L + ((qb ^ (fsw((4 * u) & 15) << 4)) + u * 4096)) = v2u{g[u].y, g[u].w};
      STAMP(2);
    }
    {
      const int go = WS_OPART + (((((lane >> 3) * 4 + (w >> 1)) * 2 + b) * 64 + s) * 256) + (w & 1) * 128 + ((lane & 7) << 4);
      const v4u z = {0u, 0u, 0u, 0u};
#pragma unroll
      for (int i = 0; i < 4; ++i) STG(z, go + i * (8 * 4 * 2 * 64 * 256));
    }
    LBAR();
    STAMP(3);
    CYC(0);
    f32x16 S;
#pragma unroll
    for (int i = 0; i < 16; ++i) S[i] = 0.f;
    {
      v4u qf[16];
#pragma unroll
      for (int st = 0; st < 16; ++st) qf[st] = lds128(L + QO + ((ob ^ ((st & 7) << 5)) + (st >> 3) * 256));
      SCHED();
#pragma unroll
      for (int st = 0; st < 16; ++st) S = mfma32(kf[st], qf[st], S);
    }
    CYC(1);
    CYC(2);
    float sv[8];
    if (ch == 0) {
      LAS f32x4* xw = (LAS f32x4*)(L + XCH + w * 2048 + lane * 16);
      xw[0] = f32x4{S[8], S[9], S[10], S[11]}; xw[64] = f32x4{S[12], S[13], S[14], S[15]};
#pragma unroll
      for (int e = 0; e < 8; ++e) sv[e] = S[e];
    } else {
      LAS f32x4* xw = (LAS f32x4*)(L + XCH + w * 2048 + lane * 16);
      xw[0] = f32x4{S[0], S[1], S[2], S[3]}; xw[64] = f32x4{S[4], S[5], S[6], S[7]};
#pragma unroll
      for (int e = 0; e < 8; ++e) sv[e] = S[8 + e];
    }
    LBAR();
    CYC(3);
    {
      LAS f32x4* xr = (LAS f32x4*)(L + XCH + (w ^ 4) * 2048 + lane * 16);
      const f32x4 p0 = xr[0], p1 = xr[64];
#pragma unroll
      for (int e = 0; e < 4; ++e) { sv[e] += p0[e]; sv[4 + e] += p1[e]; }
      float m = fmaxf(fmaxf(fmaxf(sv[0], sv[1]), fmaxf(sv[2], sv[3])), fmaxf(fmaxf(sv[4], sv[5]), fmaxf(sv[6], sv[7])));
      m = max32(m);
      *(LAS float*)(L + MX + r32 * 32 + w * 4) = m;
    }
    LBAR();
    CYC(4);
    float Mx;
    {
      const f32x4 m0 = *(LAS f32x4*)(L + MX + r32 * 32), m1 = *(LAS f32x4*)(L + MX + r32 * 32 + 16);
      Mx = fmaxf(fmaxf(fmaxf(m0[0], m0[1]), fmaxf(m0[2], m0[3])), fmaxf(fmaxf(m1[0], m1[1]), fmaxf(m1[2], m1[3])));
      float l = 0.f;
#pragma unroll
      for (int e = 0; e < 8; ++e) { sv[e] = __builtin_amdgcn_exp2f(sv[e] - Mx); l += sv[e]; }
#pragma unroll
      for (int i = 0; i < 2; ++i)
        *(LAS v2u*)(L + PB + r32 * 256 + (((4 * kt + 2 * ch + i) ^ fr) << 4) + hf * 8) = v2u{pk2(sv[4 * i], sv[4 * i + 1]), pk2(sv[4 * i + 2], sv[4 * i + 3])};
      l = sum32(l);
      *(LAS float*)(L + LS + r32 * 32 + w * 4) = l;
    }
    LBAR();
    CYC(5);
    STAMP(4);
    float Ls;
    f32x16 O0, O1;
#pragma unroll
    for (int i = 0; i < 16; ++i) { O0[i] = 0.f; O1[i] = 0.f; }
    {
      const unsigned pb = PB + r32 * 256 + ((hf ^ fr) << 4);
      v4u pf[8];
#pragma unroll
      for (int ks = 0; ks < 8; ++ks) pf[ks] = lds128(L + (pb ^ (ks << 5)));
      const f32x4 l0 = *(LAS f32x4*)(L + LS + r32 * 32), l1 = *(LAS f32x4*)(L + LS + r32 * 32 + 16);
      SCHED();
#pragma unroll
      for (int ks = 0; ks < 8; ++ks) {
        O0 = mfma32(vf[ks][0], pf[ks], O0);
        O1 = mfma32(vf[ks][1], pf[ks], O1);
      }
      Ls = ((l0[0] + l0[1]) + (l0[2] + l0[3])) + ((l1[0] + l1[1]) + (l1[2] + l1[3]));
    }
    CYC(6);
    {
      LAS char* ot = L + w * 4096;
      const unsigned ow = r32 * 128 + 8 * hf;
#pragma unroll
      for (int gq = 0; gq < 4; ++gq) {
        *(LAS v2u*)(ot + ow + ((gq ^ (r32 & 7)) << 4)) = v2u{pk2(O0[4 * gq], O0[4 * gq + 1]), pk2(O0[4 * gq + 2], O0[4 * gq + 3])};
        *(LAS v2u*)(ot + ow + (((4 + gq) ^ (r32 & 7)) << 4)) = v2u{pk2(O1[4 * gq], O1[4 * gq + 1]), pk2(O1[4 * gq + 2], O1[4 * gq + 3])};
      }
      CYC(7);
      STAMP(5);
      const int go = WS_OPART + (((((lane >> 3) * 4 + (w >> 1)) * 2 + b) * 64 + s) * 256) + (w & 1) * 128 + (((lane & 7) ^ ((lane >> 3) & 7)) << 4);
#pragma unroll
      for (int i = 0; i < 4; ++i) __builtin_amdgcn_raw_buffer_store_b128(lds128(ot + i * 1024 + lane * 16), rs, go + i * (8 * 4 * 2 * 64 * 256), 0, OAUX);
    }
    SCHED();
    CYC(8);
    asm volatile("s_waitcnt vmcnt(0)\n\ts_barrier" ::: "memory");
    CYC(9);
    if (w == 0 && lane < 32) {
      const v4u gr = {tagA, __float_as_uint(Mx), tagB, __float_as_uint(Ls)};
      STG(gr, WS_STAT + ((lane * 2 + b) * 64 + s) * 16);
    }
    CYC(10);
    STAMP(6);
    if ((a & 15) < 9) {
      const int r = (a >> 4) * 9 + (a & 15), t2 = tid - 256;
      float g0 = 0.f, g1 = 0.f, bias2 = 0.f;
      if (w == 4) {
        bias2 = bflo((unsigned)b2[8 * r + (lane & 7)]);
        float d0 = 0.f, d1 = 0.f;
#pragma unroll
        for (int i = 0; i < 2; ++i) {
          const int c = lane + 64 * i;
          if (c < 72) {
            const v4u xa = gld(x + c * 8), xb = gld(x + NM + c * 8), gg = gld(gw + c * 8);
#pragma unroll
            for (int e = 0; e < 4; ++e) {
              d0 += bflo(xa[e]) * bflo(gg[e]) + bfhi(xa[e]) * bfhi(gg[e]);
              d1 += bflo(xb[e]) * bflo(gg[e]) + bfhi(xb[e]) * bfhi(gg[e]);
            }
          }
        }
        d0 = wave_sum(d0); d1 = wave_sum(d1);
        g0 = 1.f / (1.f + __expf(-d0)); g1 = 1.f / (1.f + __expf(-d1));
      }
      const int off = WS_POUT + r * 16384 + tid * 16;
      v4u ga[2];
      wait_until(t0 + T_RED);
      for (unsigned spins = 0;; ++spins) {
        CBAR();
#pragma unroll
        for (int i = 0; i < 2; ++i) ga[i] = LDG(off + i * 8192);
        if (__all(TAGOK(ga[0]) & TAGOK(ga[1])) || spins > SPIN_CAP) break;
        __builtin_amdgcn_s_sleep(POLL_SLEEP);
      }
      STAMP(9);
      float v0 = __uint_as_float(ga[0].y) + __uint_as_float(ga[1].y);
      float v1 = __uint_as_float(ga[0].w) + __uint_as_float(ga[1].w);
      v0 += dppf<0x128>(v0); v1 += dppf<0x128>(v1);
      v0 = sum32(sum16(v0)); v1 = sum32(sum16(v1));
      if (lane < 8) { *(LAS float*)(L + 65536 + (lane * 8 + w) * 4) = v0; *(LAS float*)(L + 65536 + 256 + (lane * 8 + w) * 4) = v1; }
      LBAR();
      if (t2 >= 0 && t2 < 16) {
        const f32x4 p0 = *(LAS f32x4*)(L + 65536 + t2 * 32), p1 = *(LAS f32x4*)(L + 65536 + t2 * 32 + 16);
        const float v = ((p0[0] + p0[1]) + (p0[2] + p0[3])) + ((p1[0] + p1[1]) + (p1[2] + p1[3])) + (1.f - (t2 < 8 ? g0 : g1)) * bias2;
        out[(t2 >> 3) * NM + 8 * r + (t2 & 7)] = (u16)bf1(v);
      }
      if (r == 0 && tid == 0) __hip_atomic_store((GAS u64*)(ws + WS_EPOCH), epoch, __ATOMIC_RELAXED, __HIP_MEMORY_SCOPE_AGENT);
      STAMP(10);
    }
    STAMP_FLUSH();
  }
}

extern "C" void kernel_launch(void* const* d_in, const int* in_sizes, int n_in, void* d_out, int out_size, void* d_ws, size_t ws_size,
                              hipStream_t stream) {
  fused_kernel<<<256, 512, 0, stream>>>((const u16*)d_in[0], (const u16*)d_in[1], (const u16*)d_in[2], (const u16*)d_in[3],
                                        (const u16*)d_in[4], (const u16*)d_in[5], (const u16*)d_in[7], (const u16*)d_in[8],
                                        (const u16*)d_in[9], (const u16*)d_in[10], (const u16*)d_in[11], (u16*)d_out, (char*)d_ws + WS_BASE);
}
```

```cpp
#include <hip/hip_runtime.h>
#include <hip/hip_bf16.h>
#include <stdint.h>

#define GAS __attribute__((address_space(1)))
#define LAS __attribute__((address_space(3)))
#define DI __device__ __forceinline__
typedef unsigned short u16;
typedef unsigned long long u64;
typedef unsigned v4u __attribute__((ext_vector_type(4)));
typedef unsigned v2u __attribute__((ext_vector_type(2)));
typedef float f32x2 __attribute__((ext_vector_type(2)));
typedef float f32x4 __attribute__((ext_vector_type(4)));
typedef float f32x16 __attribute__((ext_vector_type(16)));
typedef short bf16x8 __attribute__((ext_vector_type(8)));
typedef short s16x4 __attribute__((ext_vector_type(4)));
typedef __bf16 bf2_t __attribute__((ext_vector_type(2)));

constexpr int NM = 576, NH = 32, ND = 128, NC = 512, NSK = 8192, NF = 4608;
constexpr int WS_EPOCH = 0;
constexpr int WS_HX = 4096;
constexpr int WS_QX = WS_HX + 4 * 73728;
constexpr int WS_CX = WS_QX + 262144;
constexpr int WS_QABS = WS_CX + 262144;
constexpr int WS_STAT = WS_QABS + 131072;
constexpr int WS_POUT = WS_STAT + 524288;
constexpr int WS_OPART = WS_POUT + 72 * 128 * 128;
constexpr int WS_STAMP = WS_OPART + 4194304;
constexpr int WS_BYTES = WS_STAMP + 2 * 256 * 256;
constexpr int AUX_SC1 = 16;
constexpr unsigned SPIN_CAP = 1u << 17;

#define CKV_N1 0
#define CKV_WIN 2
#define CKV_WIN2 16
#define PRETOUCH 0
#define XPOL ""
#define STAT_SW 1
#define STAT_SLEEP 2
#define OAUX 16
#define OPRE 2
#define WS_BASE 0
#define BULK_WIN 32
#define PPRE 0
#define HO0_SLEEP 3
#define T_CKV 150
#define POLL_SLEEP 2
#define T_STATS 850
#define T_HEADB 510
#define T_RED 800

#define STAMP(i) do {} while (0)
#define STAMPW(wv, i) do {} while (0)
#define STAMP_FLUSH() do {} while (0)
#define CYC(i) do {} while (0)

DI unsigned fsw(unsigned r) { return ((r & 3u) << 2) | ((((r >> 3) ^ (r >> 2)) & 1u) << 1) | ((r >> 3) & 1u); }
DI float bflo(unsigned v) { return __uint_as_float(v << 16); }
DI float bfhi(unsigned v) { return __uint_as_float(v & 0xffff0000u); }
DI unsigned pk2(float a, float b) { f32x2 f = {a, b}; bf2_t r = __builtin_convertvector(f, bf2_t); return __builtin_bit_cast(unsigned, r); }
DI unsigned bf1(float a) { return pk2(a, 0.f) & 0xffffu; }
DI f32x4 mfma16(v4u a, v4u b, f32x4 c) { return __builtin_amdgcn_mfma_f32_16x16x32_bf16(__builtin_bit_cast(bf16x8, a), __builtin_bit_cast(bf16x8, b), c, 0, 0, 0); }
DI f32x16 mfma32(v4u a, v4u b, f32x16 c) { return __builtin_amdgcn_mfma_f32_32x32x16_bf16(__builtin_bit_cast(bf16x8, a), __builtin_bit_cast(bf16x8, b), c, 0, 0, 0); }
DI v2u trread(LAS char* p) { s16x4 r = __builtin_amdgcn_ds_read_tr16_b64_v4i16((LAS s16x4*)p); return __builtin_bit_cast(v2u, r); }
DI v4u trfrag(LAS char* p0, LAS char* p1) { v2u a = trread(p0), b = trread(p1); return v4u{a.x, a.y, b.x, b.y}; }
DI v4u lds128(LAS char* p) { return *(LAS v4u*)p; }
DI void dma16(const u16* g, LAS char* l) { __builtin_amdgcn_global_load_lds((const GAS unsigned*)g, (LAS unsigned*)l, 16, 0, 2); }
DI v4u gld(const u16* p) { return *(const GAS v4u*)p; }
DI v4u gldnt(const u16* p) { return __builtin_nontemporal_load((const GAS v4u*)p); }
DI v2u gldnt8(const u16* p) { return __builtin_nontemporal_load((const GAS v2u*)p); }
template <int CTRL> DI float dppf(float x) { return __builtin_bit_cast(float, __builtin_amdgcn_mov_dpp(__builtin_bit_cast(int, x), CTRL, 0xf, 0xf, true)); }
template <int CTRL> DI unsigned dppu(unsigned x) { return (unsigned)__builtin_amdgcn_mov_dpp((int)x, CTRL, 0xf, 0xf, true); }
DI float sum32(float x) { auto t = __builtin_amdgcn_permlane32_swap(__float_as_uint(x), __float_as_uint(x), false, false); return __uint_as_float(t[0]) + __uint_as_float(t[1]); }
DI float max32(float x) { auto t = __builtin_amdgcn_permlane32_swap(__float_as_uint(x), __float_as_uint(x), false, false); return fmaxf(__uint_as_float(t[0]), __uint_as_float(t[1])); }
DI float sum16(float x) { auto t = __builtin_amdgcn_permlane16_swap(__float_as_uint(x), __float_as_uint(x), false, false); return __uint_as_float(t[0]) + __uint_as_float(t[1]); }
DI float max16(float x) { auto t = __builtin_amdgcn_permlane16_swap(__float_as_uint(x), __float_as_uint(x), false, false); return fmaxf(__uint_as_float(t[0]), __uint_as_float(t[1])); }
DI float wave_sum(float x) { x += dppf<0xB1>(x); x += dppf<0x4E>(x); x += dppf<0x141>(x); x += dppf<0x128>(x); x = sum16(x); return sum32(x); }
DI float wave_max(float x) { x = fmaxf(x, dppf<0xB1>(x)); x = fmaxf(x, dppf<0x4E>(x)); x = fmaxf(x, dppf<0x141>(x)); x = fmaxf(x, dppf<0x128>(x)); x = max16(x); return max32(x); }
DI void wait_until(u64 t) { while (__builtin_amdgcn_s_memrealtime() < t) __builtin_amdgcn_s_sleep(1); }
#define DRAIN() asm volatile("s_waitcnt vmcnt(0)" ::: "memory")
#define CBAR() asm volatile("" ::: "memory")
#define SCHED() __builtin_amdgcn_sched_barrier(0)
#define LBAR() asm volatile("s_waitcnt lgkmcnt(0)\n\ts_barrier" ::: "memory")
#define DMA16(gp, ldsoff) do { unsigned keep_; asm volatile("s_mov_b32 %0, m0\n\ts_mov_b32 m0, %2\n\ts_nop 0\n\tglobal_load_lds_dwordx4 %1, off nt\n\ts_mov_b32 m0, %0" : "=&s"(keep_) : "v"(gp), "s"((unsigned)(size_t)L + (unsigned)(ldsoff)) : "memory"); } while (0)
#define TOUCH(dst, off) asm volatile("buffer_load_dword %0, %1, %2, 0 offen sc1" : "=&v"(dst) : "v"(off), "s"(rs) : "memory")
#define DMA16X(gp, ldsoff) do { unsigned keep_; asm volatile("s_mov_b32 %0, m0\n\ts_mov_b32 m0, %2\n\ts_nop 0\n\tglobal_load_lds_dwordx4 %1, off" XPOL "\n\ts_mov_b32 m0, %0" : "=&s"(keep_) : "v"(gp), "s"((unsigned)(size_t)L + (unsigned)(ldsoff)) : "memory"); } while (0)
#define DMA16W(gp, ldsoff) do { DMA16(gp, ldsoff); if (BULK_WIN < 32) asm volatile("s_waitcnt vmcnt(%0)" :: "i"(BULK_WIN - 1) : "memory"); } while (0)
#define LDG(off) __builtin_amdgcn_raw_buffer_load_b128(rs, (off), 0, AUX_SC1)
#define STG(v, off) __builtin_amdgcn_raw_buffer_store_b128((v), rs, (off), 0, AUX_SC1)
#define TAGOK(g) (((g).x == tagA) & ((g).z == tagB))

DI int tile_lane_off(int lane, int ld) { const int r8 = lane >> 3; return (8 * (r8 >> 2) + (r8 & 3)) * ld + 8 * ((lane & 7) ^ ((r8 >> 1) << 1)); }
DI int tile_tr_off(int lane, int ct) { const int r8 = 4 * ((lane >> 4) & 1) + ((lane >> 2) & 3); return (lane >> 5) * 1024 + r8 * 128 + ((ct ^ (r8 >> 1)) * 32) + (lane & 3) * 8; }
DI void put_hilo(LAS char* base, int stride, int d, float v0, float v1) {
  const unsigned h = pk2(v0, v1);
  const unsigned l = pk2(v0 - bflo(h), v1 - bfhi(h));
  *(LAS u16*)(base + d * 2) = (u16)h;
  *(LAS u16*)(base + stride + d * 2) = (u16)(h >> 16);
  *(LAS u16*)(base + 2 * stride + d * 2) = (u16)l;
  *(LAS u16*)(base + 3 * stride + d * 2) = (u16)(l >> 16);
}

__global__ void __launch_bounds__(512, 2)
fused_kernel(const u16* __restrict__ x, const u16* __restrict__ ckv, const u16* __restrict__ wq, const u16* __restrict__ wuk,
             const u16* __restrict__ wuv, const u16* __restrict__ wo, const u16* __restrict__ w1, const u16* __restrict__ b1,
             const u16* __restrict__ w2, const u16* __restrict__ b2, const u16* __restrict__ gw, u16* __restrict__ out, char* ws) {
  __shared__ __attribute__((aligned(16))) char smem[163840];
  LAS char* L = (LAS char*)smem;
  const int tid = threadIdx.x, lane = tid & 63, w = __builtin_amdgcn_readfirstlane(tid >> 6);
  const int bid = blockIdx.x, xcd = bid & 7, kk = bid >> 3;
  const u64 t0 = __builtin_amdgcn_s_memrealtime();
  const __amdgpu_buffer_rsrc_t rs = __builtin_amdgcn_make_buffer_rsrc((void*)ws, 0, WS_BYTES, 0x00020000);
#define LOAD_EPOCH() (__hip_atomic_load((GAS u64*)(ws + WS_EPOCH), __ATOMIC_RELAXED, __HIP_MEMORY_SCOPE_AGENT) + 1ull)
  STAMP(0);
  const int g4 = lane >> 4, col = lane & 15;

  if (kk < 16) {
    const int h = xcd * 4 + (kk >> 2), j = kk & 3;
    constexpr int WQ = 0, WUK = 40960, W1 = 69632, W2 = W1 + 40960, W2X = W2 + 36864, WUV = 0, WO = 32768;
    constexpr int RED = W2X + 9216, CTXA = RED + 4096, QA = CTXA + 1024, PRIV = RED, HR = QA + 512;
    const int ks0 = j < 2 ? 5 * j : 4 * j + 2;
    u64 epoch;
    asm volatile("global_load_dwordx2 %0, %1, off sc1" : "=&v"(epoch) : "v"(ws + WS_EPOCH) : "memory");
    constexpr int XG = W2X;
    if (w < 3) { const int c = 64 * w + lane; DMA16X(x + 8 * (c < 144 ? c : 0), XG + w * 1024); }
    else if (w < 5) { const int c = 64 * (w - 3) + lane; DMA16X(gw + 8 * (c < 72 ? c : 0), XG + w * 1024); }
    {
      const u16* src = wq + (size_t)(32 * ks0) * 4096 + h * ND + tile_lane_off(lane, 4096) + (16 * (w & 1) + 4 * (w >> 2)) * 4096 + 64 * ((w >> 1) & 1);
#pragma unroll
      for (int i = 0; i < 5; ++i)
        if (i < 4 || j < 2) DMA16(src + (size_t)i * 32 * 4096, WQ + (i * 8 + w) * 1024);
#pragma unroll
      for (int i = 0; i < 4; ++i) {
        const int row = 16 * w + 4 * i + g4;
        DMA16(wuk + (size_t)(128 * j + row) * 4096 + h * ND + 8 * (col ^ fsw(4 * i + g4)), WUK + (4 * w + i) * 1024);
      }
    }
    asm volatile("s_waitcnt vmcnt(4)\n\ts_barrier" : "+v"(epoch) :: "memory");
    v4u xa[5];
#pragma unroll
    for (int i = 0; i < 5; ++i) xa[i] = lds128(L + XG + (col & 1) * 1152 + 64 * (ks0 + ((i < 4 || j < 2) ? i : 0)) + 16 * g4);
    epoch += 1ull;
    if ((unsigned)epoch == 0u) epoch += 1ull;
    const unsigned tagA = (unsigned)epoch, tagB = (unsigned)(epoch >> 32);
    STAMP(1);
    {
      f32x4 acc = {0.f, 0.f, 0.f, 0.f};
      LAS char* p = L + WQ + (w >> 2) * 2048 + tile_tr_off(lane, w & 3);
      v4u bq[5];
#pragma unroll
      for (int i = 0; i < 4; ++i) bq[i] = trfrag(p + i * 8192, p + i * 8192 + 4096);
      if (j < 2) bq[4] = trfrag(p + 4 * 8192, p + 4 * 8192 + 4096);
#pragma unroll
      for (int i = 0; i < 4; ++i) acc = mfma16(xa[i], bq[i], acc);
      if (j < 2) acc = mfma16(xa[4], bq[4], acc);
      if (g4 == 0) { const v4u gr = {tagA, __float_as_uint(acc[0]), tagB, __float_as_uint(acc[1])}; STG(gr, WS_QX + ((h * 4 + j) * 128 + 16 * w + col) * 16); }
    }
    STAMP(2);
    float g0, g1;
    {
      const int gc = lane + 64 < 72 ? lane + 64 : 0;
      const v4u gq[6] = {lds128(L + XG + lane * 16), lds128(L + XG + 1152 + lane * 16), lds128(L + XG + 3072 + lane * 16),
                         lds128(L + XG + gc * 16), lds128(L + XG + 1152 + gc * 16), lds128(L + XG + 3072 + gc * 16)};
      float d0 = 0.f, d1 = 0.f, e0 = 0.f, e1 = 0.f;
#pragma unroll
      for (int e = 0; e < 4; ++e) {
        d0 += bflo(gq[0][e]) * bflo(gq[2][e]) + bfhi(gq[0][e]) * bfhi(gq[2][e]);
        d1 += bflo(gq[1][e]) * bflo(gq[2][e]) + bfhi(gq[1][e]) * bfhi(gq[2][e]);
        e0 += bflo(gq[3][e]) * bflo(gq[5][e]) + bfhi(gq[3][e]) * bfhi(gq[5][e]);
        e1 += bflo(gq[4][e]) * bflo(gq[5][e]) + bfhi(gq[4][e]) * bfhi(gq[5][e]);
      }
      if (lane < 8) { d0 += e0; d1 += e1; }
      d0 = wave_sum(d0); d1 = wave_sum(d1);
      g0 = 1.f / (1.f + __expf(-d0)); g1 = 1.f / (1.f + __expf(-d1));
      asm volatile("" : "+v"(g0), "+v"(g1));
    }
    DRAIN();
    __builtin_amdgcn_s_sleep(HO0_SLEEP);
    LAS char* pv = L + PRIV + w * 256;
    {
      v4u ga;
      const int off = WS_QX + ((h * 4 + g4) * 128 + 16 * w + col) * 16;
      for (unsigned spins = 0;; ++spins) {
        CBAR();
        ga = LDG(off);
        if (__all(TAGOK(ga)) || spins > SPIN_CAP) break;
        __builtin_amdgcn_s_sleep(1);
      }
      STAMP(3);
      v4u uk[4];
#pragma unroll
      for (int ks = 0; ks < 4; ++ks) uk[ks] = lds128(L + WUK + (16 * w + col) * 256 + (((4 * ks + g4) ^ fsw(col)) << 4));
      const float q0 = sum32(sum16(__uint_as_float(ga.y))), q1 = sum32(sum16(__uint_as_float(ga.w)));
      const float qv = (g4 & 1) ? q1 : q0;
      const unsigned hi = bf1(qv);
      *(LAS u16*)(L + QA + g4 * 256 + (16 * w + col) * 2) = (u16)((g4 & 2) ? bf1(qv - bflo(hi)) : hi);
      LBAR();
      f32x4 a = {0.f, 0.f, 0.f, 0.f};
#pragma unroll
      for (int ks = 0; ks < 4; ++ks) a = mfma16(lds128(L + QA + (col & 3) * 256 + ks * 64 + g4 * 16), uk[ks], a);
      const float v = ((g4 & 1) ? (a[1] + a[3]) : (a[0] + a[2])) * (0.08838834764831845f * 1.4426950408889634f);
      const unsigned bits = bf1(v);
      const unsigned n1 = dppu<0x55>(bits), n2 = dppu<0xAA>(bits), n3 = dppu<0xFF>(bits);
      if ((lane & 3) == 0 && g4 < 2) {
        const v4u gr = {tagA, bits | (n1 << 16), tagB, n2 | (n3 << 16)};
        STG(gr, WS_QABS + ((g4 * 32 + h) * 128 + 32 * j + 4 * w + (col >> 2)) * 16);
      }
    }
    STAMP(4);
    const int pidx = h * 4 + j;
    const bool dbl = kk >= 14;
    const int u0 = (xcd & 1) ? (dbl ? 12 + 4 * (kk - 14) : kk < 12 ? kk : kk + 2) : (dbl ? 10 + 4 * (kk - 14) : kk < 10 ? kk : kk < 12 ? kk + 2 : kk + 4);
    const int r2 = dbl ? 4480 + 8 * (xcd * 2 + kk - 14) : 40 * (xcd * 14 + kk);
    const int nk2 = dbl ? 8 : 40;
    unsigned b1v[3];
    {
      const int ug = xcd * 18 + u0, uq = ug & 3, f0 = 128 * (ug >> 2);
      const int kq0 = uq < 2 ? 5 * uq : 4 * uq + 2, kq1 = 14;
      v4u xm[2][5];
#pragma unroll
      for (int i = 0; i < 5; ++i) {
        const int ki = (i < 4 || uq < 2) ? i : 0;
        xm[0][i] = lds128(L + XG + (col & 1) * 1152 + 64 * (kq0 + ki) + 16 * g4);
        xm[1][i] = lds128(L + XG + (col & 1) * 1152 + 64 * (kq1 + ki) + 16 * g4);
      }
      LBAR();
      if (T_HEADB > 0) wait_until(t0 + T_HEADB);
      {
        const u16* src = wuv + (size_t)(128 * j) * 4096 + h * ND + tile_lane_off(lane, 4096) + (16 * (w & 1) + 4 * (w >> 2)) * 4096 + 64 * ((w >> 1) & 1);
#pragma unroll
        for (int i = 0; i < 4; ++i) DMA16W(src + (size_t)i * 32 * 4096, WUV + (i * 8 + w) * 1024);
        const u16* so = wo + (size_t)(h * ND + 32 * j) * NM + tile_lane_off(lane, NM) + (16 * (w & 1) + 4 * ((w >> 1) & 1)) * NM + 64 * (w >> 2);
#pragma unroll
        for (int i = 0; i < 5; ++i)
          if (i < 4 || w < 4) DMA16W(so + 128 * i, WO + (i * 8 + w) * 1024);
      }
      {
        const int r8 = lane >> 3;
        const u16* s3 = w2 + (size_t)(r2 + nk2 - 8 + r8) * NM + 8 * ((lane & 7) ^ ((r8 >> 1) << 1));
        DMA16W(s3 + 64 * w, W2X + w * 1024);
        if (w == 0) DMA16W(s3 + 512, W2X + 8192);
      }
      if (!dbl) {
        const u16* s2 = w2 + (size_t)r2 * NM + tile_lane_off(lane, NM) + (16 * (w & 1) + 4 * ((w >> 1) & 1)) * NM + 64 * (w >> 2);
#pragma unroll
        for (int i = 0; i < 5; ++i)
          if (i < 4 || w < 4) DMA16W(s2 + 128 * i, W2 + (i * 8 + w) * 1024);
      }
      {
        const int fb = r2 + col;
        asm volatile("global_load_ushort %0, %3, off\n\tglobal_load_ushort %1, %4, off\n\tglobal_load_ushort %2, %5, off"
                     : "=&v"(b1v[0]), "=&v"(b1v[1]), "=&v"(b1v[2])
                     : "v"(b1 + (col < nk2 ? fb : r2)), "v"(b1 + (16 + col < nk2 ? fb + 16 : r2)), "v"(b1 + (32 + col < nk2 ? fb + 32 : r2)) : "memory");
      }
      {
        const u16* s1 = w1 + f0 + tile_lane_off(lane, NF) + (size_t)(16 * (w & 1) + 4 * (w >> 2)) * NF + 64 * ((w >> 1) & 1);
#pragma unroll
        for (int i = 0; i < 5; ++i)
          if (i < 4 || uq < 2) DMA16W(s1 + (size_t)(kq0 + i) * 32 * NF, W1 + (i * 8 + w) * 1024);
        if (dbl) {
#pragma unroll
          for (int i = 0; i < 4; ++i) DMA16W(s1 + (size_t)(kq1 + i) * 32 * NF, W2 + (i * 8 + w) * 1024);
        }
      }
      asm volatile("s_waitcnt vmcnt(0)\n\ts_barrier" : "+v"(b1v[0]), "+v"(b1v[1]), "+v"(b1v[2]) :: "memory");
      STAMP(10);
#pragma unroll
      for (int un = 0; un < 2; ++un) {
        if (un == 0 || dbl) {
          LAS char* p = L + (un == 0 ? W1 : W2) + (w >> 2) * 2048 + tile_tr_off(lane, w & 3);
          f32x4 acc = {0.f, 0.f, 0.f, 0.f};
#pragma unroll
          for (int i = 0; i < 4; ++i) acc = mfma16(xm[un][i], trfrag(p + i * 8192, p + i * 8192 + 4096), acc);
          if (uq < 2) acc = mfma16(xm[un][4], trfrag(p + 4 * 8192, p + 4 * 8192 + 4096), acc);
          if (g4 == 0) {
            const v4u gr = {tagA, __float_as_uint(acc[0]), tagB, __float_as_uint(acc[1])};
            STG(gr, WS_HX + ((uq + un) * NF + f0 + 16 * w + col) * 16);
          }
        }
      }
      STAMP(11);
    }
    v4u uv[4], wf[5];
    {
      LAS char* p = L + WUV + (w >> 2) * 2048 + tile_tr_off(lane, w & 3);
#pragma unroll
      for (int i = 0; i < 4; ++i) uv[i] = trfrag(p + i * 8192, p + i * 8192 + 4096);
      LAS char* po = L + WO + (w >> 2) * 4096 + tile_tr_off(lane, w & 3);
#pragma unroll
      for (int i = 0; i < 5; ++i) wf[i] = (i < 4 || w < 4) ? trfrag(po + i * 8192, po + i * 8192 + 2048) : v4u{0u, 0u, 0u, 0u};
    }
    v4u w2m[5], w2x[5];
    {
      const int q = (lane >> 2) & 3;
      LAS char* p = L + W2X + (w >> 2) * 1024 + q * 128 + (lane & 3) * 8;
      const int o0 = (((w & 3) ^ (q >> 1)) * 32), o1 = 512 + (((w & 3) ^ (2 + (q >> 1))) * 32);
      LAS char* pm = L + W2 + (w >> 2) * 4096 + tile_tr_off(lane, w & 3);
#pragma unroll
      for (int i = 0; i < 5; ++i) {
        const int ii = (i < 4 || w < 4) ? i : 0;
        w2x[i] = trfrag(p + ii * 2048 + o0, p + ii * 2048 + o1);
        w2m[i] = trfrag(pm + ii * 8192, pm + ii * 8192 + 2048);
        if (dbl) w2m[i] = v4u{0u, 0u, 0u, 0u};
      }
    }
    STAMP(5);
    const int b = w >> 2, q4 = w & 3;
    float wgt;
    v4u hg = {0u, 0u, 0u, 0u}, cxw = {0u, 0u, 0u, 0u};
    const int ht = w - 4, hf_ = 16 * ht + col;
    const int ho = WS_HX + (g4 * NF + r2 + (hf_ < nk2 ? hf_ : 0)) * 16;
    constexpr int HA = QA;
    {
      const int off = WS_STAT + ((h * 2 + b) * 64 + 16 * q4 + ((lane >> 1) & 15)) * 16;
      const int ooff = WS_OPART + ((((h * 4 + j) * 2 + b) * 64 + 16 * q4) * 256) + lane * 16;
      v4u g, o[4];
      bool ok = lane >= 32;
      unsigned done = 0;
      if (w < 2) { cxw = LDG(WS_CX + ((h * 4 + j) * 128 + w * 64 + lane) * 16); asm volatile("" :: "v"(cxw)); }
      wait_until(t0 + T_STATS);
      for (unsigned spins = 0;; ++spins) {
        CBAR();
        if (!ok) { g = LDG(off); ok = TAGOK(g); }
        const unsigned m = (unsigned)__builtin_amdgcn_ballot_w64(ok) | (spins > SPIN_CAP ? 0xffffffffu : 0u);
        __builtin_amdgcn_fence(__ATOMIC_ACQUIRE, "wavefront");
#pragma unroll
        for (int i = 0; i < 4; ++i)
          if (!(done & (1u << i)) && ((m >> (8 * i)) & 0xffu) == 0xffu) { o[i] = LDG(ooff + i * 1024); done |= 1u << i; }
        if (done == 15u) break;
        __builtin_amdgcn_s_sleep(STAT_SLEEP);
      }
      if (ht >= 0 && ht < 3) hg = LDG(ho);
      STAMPW(0, 6);
      const float ms = lane < 32 ? __uint_as_float(g.y) : -INFINITY, ls = (lane < 32 && !(lane & 1)) ? __uint_as_float(g.w) : 0.f;
      float Mw = fmaxf(ms, dppf<0x4E>(ms));
      Mw = fmaxf(Mw, dppf<0x141>(Mw)); Mw = fmaxf(Mw, dppf<0x128>(Mw)); Mw = max32(max16(Mw));
      wgt = __builtin_amdgcn_exp2f(ms - Mw);
      float ww[4];
#pragma unroll
      for (int i = 0; i < 4; ++i) ww[i] = __uint_as_float((unsigned)__builtin_amdgcn_ds_bpermute((4 * i + g4) * 8, (int)__float_as_uint(wgt)));
      float Lw = wgt * ls;
      Lw += dppf<0xB1>(Lw); Lw += dppf<0x4E>(Lw); Lw += dppf<0x141>(Lw); Lw += dppf<0x128>(Lw); Lw = sum32(sum16(Lw));
      if (lane == 0) *(LAS f32x2*)(L + HR + w * 8) = f32x2{Mw, Lw};
      asm volatile("" : "+v"(ww[0]), "+v"(ww[1]), "+v"(ww[2]), "+v"(ww[3]));
      SCHED();
      float acc[8] = {0.f, 0.f, 0.f, 0.f, 0.f, 0.f, 0.f, 0.f};
#pragma unroll
      for (int i = 0; i < 4; ++i) {
        acc[0] += ww[i] * bflo(o[i].x); acc[1] += ww[i] * bfhi(o[i].x);
        acc[2] += ww[i] * bflo(o[i].y); acc[3] += ww[i] * bfhi(o[i].y);
        acc[4] += ww[i] * bflo(o[i].z); acc[5] += ww[i] * bfhi(o[i].z);
        acc[6] += ww[i] * bflo(o[i].w); acc[7] += ww[i] * bfhi(o[i].w);
      }
      float r4[4];
#pragma unroll
      for (int e = 0; e < 4; ++e) {
        auto t = __builtin_amdgcn_permlane32_swap(__float_as_uint(acc[e]), __float_as_uint(acc[4 + e]), false, false);
        r4[e] = sum16(__uint_as_float(t[0]) + __uint_as_float(t[1]));
      }
      if ((g4 & 1) == 0) *(LAS f32x4*)(L + RED + w * 512 + col * 32 + (lane >> 5) * 16) = f32x4{r4[0], r4[1], r4[2], r4[3]};
    }
    LBAR();
    if (tid < 256) {
      const int bb_ = tid >> 7, c = tid & 127;
      LAS float* r = (LAS float*)(L + RED + bb_ * 2048) + c;
      const f32x4 s0 = *(LAS f32x4*)(L + HR + bb_ * 32), s1 = *(LAS f32x4*)(L + HR + bb_ * 32 + 16);
      const float r0 = r[0], r1 = r[128], r2_ = r[256], r3 = r[384];
      const float Ma = fmaxf(fmaxf(s0[0], s0[2]), fmaxf(s1[0], s1[2]));
      const float a0 = __builtin_amdgcn_exp2f(s0[0] - Ma), a1 = __builtin_amdgcn_exp2f(s0[2] - Ma), a2 = __builtin_amdgcn_exp2f(s1[0] - Ma), a3 = __builtin_amdgcn_exp2f(s1[2] - Ma);
      const float ia = __builtin_amdgcn_rcpf((a0 * s0[1] + a1 * s0[3]) + (a2 * s1[1] + a3 * s1[3]));
      const float v = ((a0 * r0 + a1 * r1) + (a2 * r2_ + a3 * r3)) * ia;
      const unsigned hi = bf1(v);
      *(LAS u16*)(L + CTXA + bb_ * 256 + c * 2) = (u16)hi;
      *(LAS u16*)(L + CTXA + (2 + bb_) * 256 + c * 2) = (u16)bf1(v - bflo(hi));
    } else if (w < 7) {
      for (unsigned spins = 0; !__all(TAGOK(hg)) && spins < SPIN_CAP; ++spins) {
        __builtin_amdgcn_s_sleep(POLL_SLEEP);
        CBAR();
        hg = LDG(ho);
      }
      const float bb = bflo(ht == 0 ? b1v[0] : ht == 1 ? b1v[1] : b1v[2]);
      const float p0 = fmaxf(sum32(sum16(__uint_as_float(hg.y))) + bb, 0.f), p1 = fmaxf(sum32(sum16(__uint_as_float(hg.w))) + bb, 0.f);
      const float hv = hf_ < nk2 ? ((g4 & 1) ? p1 * p1 : p0 * p0) : 0.f;
      const unsigned hi = bf1(hv);
      const int kpos = dbl ? 32 - 16 * ht + col : hf_;
      *(LAS u16*)(L + HA + g4 * 128 + kpos * 2) = (u16)((g4 & 2) ? bf1(hv - bflo(hi)) : hi);
    } else {
      *(LAS u16*)(L + HA + g4 * 128 + (48 + col) * 2) = 0;
    }
    LBAR();
    {
      v4u af[4];
#pragma unroll
      for (int i = 0; i < 4; ++i) af[i] = lds128(L + CTXA + (col & 3) * 256 + i * 64 + g4 * 16);
      SCHED();
      f32x4 acc = mfma16(af[1], uv[1], mfma16(af[0], uv[0], f32x4{0.f, 0.f, 0.f, 0.f}));
      acc += mfma16(af[3], uv[3], mfma16(af[2], uv[2], f32x4{0.f, 0.f, 0.f, 0.f}));
      if (g4 == 0) {
        const v4u gr = {tagA, __float_as_uint(acc[0] + acc[2]), tagB, __float_as_uint(acc[1] + acc[3])};
        STG(gr, WS_CX + (((h * 4 + (w >> 1)) * 4 + j) * 32 + 16 * (w & 1) + col) * 16);
      }
    }
    STAMP(7);
    f32x2 mpa, mpb;
    {
      const v4u ha0 = lds128(L + HA + (col & 3) * 128 + g4 * 16), hax = lds128(L + HA + (col & 3) * 128 + 64 + g4 * 16);
      f32x4 acc[5];
#pragma unroll
      for (int i = 0; i < 5; ++i) acc[i] = mfma16(ha0, w2m[i], mfma16(hax, w2x[i], f32x4{0.f, 0.f, 0.f, 0.f}));
      const f32x4 a = g4 == 0 ? acc[0] : g4 == 1 ? acc[1] : g4 == 2 ? acc[2] : acc[3];
      mpa = f32x2{(1.f - g0) * (a[0] + a[2]), (1.f - g1) * (a[1] + a[3])};
      mpb = f32x2{(1.f - g0) * (acc[4][0] + acc[4][2]), (1.f - g1) * (acc[4][1] + acc[4][3])};
    }
    {
      v4u ga, gb;
      const int off = WS_CX + ((h * 4 + j) * 128 + lane) * 16;
      for (unsigned spins = 0;; ++spins) {
        CBAR();
        ga = LDG(off); gb = LDG(off + 1024);
        if (__all(TAGOK(ga) & TAGOK(gb)) || spins > SPIN_CAP) break;
        __builtin_amdgcn_s_sleep(1);
      }
      STAMP(8);
      const float v0 = sum32(__uint_as_float(ga.y) + __uint_as_float(gb.y)), v1 = sum32(__uint_as_float(ga.w) + __uint_as_float(gb.w));
      if (lane < 32) put_hilo(pv, 64, lane, v0, v1);
      const v4u ca = lds128(pv + (col & 3) * 64 + g4 * 16);
      f32x4 acc[5];
#pragma unroll
      for (int i = 0; i < 5; ++i) acc[i] = mfma16(ca, wf[i], f32x4{0.f, 0.f, 0.f, 0.f});
      const f32x4 a = g4 == 0 ? acc[0] : g4 == 1 ? acc[1] : g4 == 2 ? acc[2] : acc[3];
      {
        const int ct = w + 8 * g4;
        const f32x2 mp = mpa;
        const v4u gr = {tagA, __float_as_uint(g0 * (a[0] + a[2]) + mp.x), tagB, __float_as_uint(g1 * (a[1] + a[3]) + mp.y)};
        STG(gr, WS_POUT + ((2 * ct + (col >> 3)) * 128 + pidx) * 128 + (col & 7) * 16);
      }
      if (w < 4 && g4 == 0) {
        const int ct = w + 32;
        const f32x2 mp = mpb;
        const v4u gr = {tagA, __float_as_uint(g0 * (acc[4][0] + acc[4][2]) + mp.x), tagB, __float_as_uint(g1 * (acc[4][1] + acc[4][3]) + mp.y)};
        STG(gr, WS_POUT + ((2 * ct + (col >> 3)) * 128 + pidx) * 128 + (col & 7) * 16);
      }
    }
    STAMP(9);
    STAMP_FLUSH();
    __builtin_amdgcn_endpgm();
  } else {
    const int a = xcd * 16 + (kk - 16), b = a >> 6, s = a & 63;
    constexpr int QO = 131072, XCH = 32768, PB = XCH + 16384, MX = XCH + 24576, LS = XCH + 25600;
    u64 epoch;
    asm volatile("global_load_dwordx2 %0, %1, off sc1" : "=&v"(epoch) : "v"(ws + WS_EPOCH) : "memory");
    {
      const u16* src = ckv + ((size_t)b * NSK + 128 * s + 16 * w) * NC;
#pragma unroll
      for (int i = 0; i < 16; ++i) {
        if (i == CKV_N1) wait_until(t0 + T_CKV);
        DMA16(src + i * NC + ((lane ^ fsw(i)) * 8), (16 * w + i) * 1024);
        if (i < CKV_N1 && i >= CKV_WIN - 1) asm volatile("s_waitcnt vmcnt(%0)" :: "i"(CKV_WIN - 1) : "memory");
        if (i >= CKV_N1 && i < 15 && CKV_WIN2 < 16) asm volatile("s_waitcnt vmcnt(%0)" :: "i"(CKV_WIN2 - 1) : "memory");
      }
    }
    const int r32 = lane & 31, hf = lane >> 5, kt = w & 3, ch = w >> 2;
    const unsigned fr = fsw(r32 & 15);
    const unsigned ob = r32 * 1024 + ch * 512 + ((hf ^ fr) << 4);
    asm volatile("s_waitcnt vmcnt(0)\n\ts_barrier" : "+v"(epoch) :: "memory");
    epoch += 1ull;
    if ((unsigned)epoch == 0u) epoch += 1ull;
    const unsigned tagA = (unsigned)epoch, tagB = (unsigned)(epoch >> 32);
    STAMP(1);
    v4u kf[16], vf[8][2];
    {
      const int gl = (w & 1) * 64 + lane, hw = w >> 1;
      const int off = WS_QABS + ((b * 32 + hw) * 128 + gl) * 16;
      v4u g[8];
#pragma unroll
      for (int u = 0; u < 8; ++u) g[u] = LDG(off + u * 8192);
      SCHED();
#pragma unroll
      for (int st = 0; st < 16; ++st) kf[st] = lds128(L + kt * 32768 + ((ob ^ ((st & 7) << 5)) + (st >> 3) * 256));
      const int blk = (lane >> 4) & 1, q = (lane >> 2) & 3, p = lane & 3;
      unsigned va[2][2];
#pragma unroll
      for (int t = 0; t < 2; ++t)
#pragma unroll
        for (int cc = 0; cc < 2; ++cc)
          va[t][cc] = (8 * hf + 4 * t + q) * 1024 + (((8 * w + 4 * cc + 2 * blk + (p >> 1)) ^ (4 * q + 2 * (hf ^ t) + hf)) << 4) + 8 * (p & 1);
#pragma unroll
      for (int ks = 0; ks < 8; ++ks)
#pragma unroll
        for (int cc = 0; cc < 2; ++cc) vf[ks][cc] = trfrag(L + va[0][cc] + ks * 16384, L + va[1][cc] + ks * 16384);
      SCHED();
      unsigned todo = 0;
#pragma unroll
      for (int u = 0; u < 8; ++u) if (!__all(TAGOK(g[u]))) todo |= 1u << u;
      for (unsigned spins = 0; todo != 0 && spins < SPIN_CAP; ++spins) {
        __builtin_amdgcn_s_sleep(POLL_SLEEP);
        CBAR();
#pragma unroll
        for (int u = 0; u < 8; ++u) if (todo & (1u << u)) g[u] = LDG(off + u * 8192);
#pragma unroll
        for (int u = 0; u < 8; ++u) if (todo & (1u << u)) { if (__all(TAGOK(g[u]))) todo &= ~(1u << u); }
      }
      const unsigned qb = QO + hw * 1024 + (((gl >> 1) ^ (hw << 2)) << 4) + (gl & 1) * 8;
#pragma unroll
      for (int u = 0; u < 8; ++u) *(LAS v2u*)(L + ((qb ^ (fsw((4 * u) & 15) << 4)) + u * 4096)) = v2u{g[u].y, g[u].w};
      STAMP(2);
    }
    {
      const int go = WS_OPART + (((((lane >> 3) * 4 + (w >> 1)) * 2 + b) * 64 + s) * 256) + (w & 1) * 128 + ((lane & 7) << 4);
      const v4u z = {0u, 0u, 0u, 0u};
#pragma unroll
      for (int i = 0; i < 4; ++i) STG(z, go + i * (8 * 4 * 2 * 64 * 256));
    }
    LBAR();
    STAMP(3);
    CYC(0);
    f32x16 S;
#pragma unroll
    for (int i = 0; i < 16; ++i) S[i] = 0.f;
    {
      v4u qf[16];
#pragma unroll
      for (int st = 0; st < 16; ++st) qf[st] = lds128(L + QO + ((ob ^ ((st & 7) << 5)) + (st >> 3) * 256));
      SCHED();
#pragma unroll
      for (int st = 0; st < 16; ++st) S = mfma32(kf[st], qf[st], S);
    }
    CYC(1);
    CYC(2);
    float sv[8];
    if (ch == 0) {
      LAS f32x4* xw = (LAS f32x4*)(L + XCH + w * 2048 + lane * 16);
      xw[0] = f32x4{S[8], S[9], S[10], S[11]}; xw[64] = f32x4{S[12], S[13], S[14], S[15]};
#pragma unroll
      for (int e = 0; e < 8; ++e) sv[e] = S[e];
    } else {
      LAS f32x4* xw = (LAS f32x4*)(L + XCH + w * 2048 + lane * 16);
      xw[0] = f32x4{S[0], S[1], S[2], S[3]}; xw[64] = f32x4{S[4], S[5], S[6], S[7]};
#pragma unroll
      for (int e = 0; e < 8; ++e) sv[e] = S[8 + e];
    }
    LBAR();
    CYC(3);
    {
      LAS f32x4* xr = (LAS f32x4*)(L + XCH + (w ^ 4) * 2048 + lane * 16);
      const f32x4 p0 = xr[0], p1 = xr[64];
#pragma unroll
      for (int e = 0; e < 4; ++e) { sv[e] += p0[e]; sv[4 + e] += p1[e]; }
      float m = fmaxf(fmaxf(fmaxf(sv[0], sv[1]), fmaxf(sv[2], sv[3])), fmaxf(fmaxf(sv[4], sv[5]), fmaxf(sv[6], sv[7])));
      m = max32(m);
      *(LAS float*)(L + MX + r32 * 32 + w * 4) = m;
    }
    LBAR();
    CYC(4);
    float Mx;
    {
      const f32x4 m0 = *(LAS f32x4*)(L + MX + r32 * 32), m1 = *(LAS f32x4*)(L + MX + r32 * 32 + 16);
      Mx = fmaxf(fmaxf(fmaxf(m0[0], m0[1]), fmaxf(m0[2], m0[3])), fmaxf(fmaxf(m1[0], m1[1]), fmaxf(m1[2], m1[3])));
      float l = 0.f;
#pragma unroll
      for (int e = 0; e < 8; ++e) { sv[e] = __builtin_amdgcn_exp2f(sv[e] - Mx); l += sv[e]; }
#pragma unroll
      for (int i = 0; i < 2; ++i)
        *(LAS v2u*)(L + PB + r32 * 256 + (((4 * kt + 2 * ch + i) ^ fr) << 4) + hf * 8) = v2u{pk2(sv[4 * i], sv[4 * i + 1]), pk2(sv[4 * i + 2], sv[4 * i + 3])};
      l = sum32(l);
      *(LAS float*)(L + LS + r32 * 32 + w * 4) = l;
    }
    LBAR();
    CYC(5);
    STAMP(4);
    float Ls;
    f32x16 O0, O1;
#pragma unroll
    for (int i = 0; i < 16; ++i) { O0[i] = 0.f; O1[i] = 0.f; }
    {
      const unsigned pb = PB + r32 * 256 + ((hf ^ fr) << 4);
      v4u pf[8];
#pragma unroll
      for (int ks = 0; ks < 8; ++ks) pf[ks] = lds128(L + (pb ^ (ks << 5)));
      const f32x4 l0 = *(LAS f32x4*)(L + LS + r32 * 32), l1 = *(LAS f32x4*)(L + LS + r32 * 32 + 16);
      SCHED();
#pragma unroll
      for (int ks = 0; ks < 8; ++ks) {
        O0 = mfma32(vf[ks][0], pf[ks], O0);
        O1 = mfma32(vf[ks][1], pf[ks], O1);
      }
      Ls = ((l0[0] + l0[1]) + (l0[2] + l0[3])) + ((l1[0] + l1[1]) + (l1[2] + l1[3]));
    }
    CYC(6);
    {
      LAS char* ot = L + w * 4096;
      const unsigned ow = r32 * 128 + 8 * hf;
#pragma unroll
      for (int gq = 0; gq < 4; ++gq) {
        *(LAS v2u*)(ot + ow + ((gq ^ (r32 & 7)) << 4)) = v2u{pk2(O0[4 * gq], O0[4 * gq + 1]), pk2(O0[4 * gq + 2], O0[4 * gq + 3])};
        *(LAS v2u*)(ot + ow + (((4 + gq) ^ (r32 & 7)) << 4)) = v2u{pk2(O1[4 * gq], O1[4 * gq + 1]), pk2(O1[4 * gq + 2], O1[4 * gq + 3])};
      }
      CYC(7);
      STAMP(5);
      const int go = WS_OPART + (((((lane >> 3) * 4 + (w >> 1)) * 2 + b) * 64 + s) * 256) + (w & 1) * 128 + (((lane & 7) ^ ((lane >> 3) & 7)) << 4);
#pragma unroll
      for (int i = 0; i < 4; ++i) __builtin_amdgcn_raw_buffer_store_b128(lds128(ot + i * 1024 + lane * 16), rs, go + i * (8 * 4 * 2 * 64 * 256), 0, OAUX);
    }
    SCHED();
    CYC(8);
    asm volatile("s_waitcnt vmcnt(0)\n\ts_barrier" ::: "memory");
    CYC(9);
    if (w == 0 && lane < 32) {
      const v4u gr = {tagA, __float_as_uint(Mx), tagB, __float_as_uint(Ls)};
      STG(gr, WS_STAT + ((lane * 2 + b) * 64 + s) * 16);
    }
    CYC(10);
    STAMP(6);
    if ((a & 15) < 9) {
      const int r = (a >> 4) * 9 + (a & 15), t2 = tid - 256;
      float g0 = 0.f, g1 = 0.f, bias2 = 0.f;
      if (w == 4) {
        bias2 = bflo((unsigned)b2[8 * r + (lane & 7)]);
        float d0 = 0.f, d1 = 0.f;
#pragma unroll
        for (int i = 0; i < 2; ++i) {
          const int c = lane + 64 * i;
          if (c < 72) {
            const v4u xa = gld(x + c * 8), xb = gld(x + NM + c * 8), gg = gld(gw + c * 8);
#pragma unroll
            for (int e = 0; e < 4; ++e) {
              d0 += bflo(xa[e]) * bflo(gg[e]) + bfhi(xa[e]) * bfhi(gg[e]);
              d1 += bflo(xb[e]) * bflo(gg[e]) + bfhi(xb[e]) * bfhi(gg[e]);
            }
          }
        }
        d0 = wave_sum(d0); d1 = wave_sum(d1);
        g0 = 1.f / (1.f + __expf(-d0)); g1 = 1.f / (1.f + __expf(-d1));
      }
      const int off = WS_POUT + r * 16384 + tid * 16;
      v4u ga[2];
      wait_until(t0 + T_RED);
      for (unsigned spins = 0;; ++spins) {
        CBAR();
#pragma unroll
        for (int i = 0; i < 2; ++i) ga[i] = LDG(off + i * 8192);
        if (__all(TAGOK(ga[0]) & TAGOK(ga[1])) || spins > SPIN_CAP) break;
        __builtin_amdgcn_s_sleep(POLL_SLEEP);
      }
      STAMP(9);
      float v0 = __uint_as_float(ga[0].y) + __uint_as_float(ga[1].y);
      float v1 = __uint_as_float(ga[0].w) + __uint_as_float(ga[1].w);
      v0 += dppf<0x128>(v0); v1 += dppf<0x128>(v1);
      v0 = sum32(sum16(v0)); v1 = sum32(sum16(v1));
      if (lane < 8) { *(LAS float*)(L + 65536 + (lane * 8 + w) * 4) = v0; *(LAS float*)(L + 65536 + 256 + (lane * 8 + w) * 4) = v1; }
      LBAR();
      if (t2 >= 0 && t2 < 16) {
        const f32x4 p0 = *(LAS f32x4*)(L + 65536 + t2 * 32), p1 = *(LAS f32x4*)(L + 65536 + t2 * 32 + 16);
        const float v = ((p0[0] + p0[1]) + (p0[2] + p0[3])) + ((p1[0] + p1[1]) + (p1[2] + p1[3])) + (1.f - (t2 < 8 ? g0 : g1)) * bias2;
        out[(t2 >> 3) * NM + 8 * r + (t2 & 7)] = (u16)bf1(v);
      }
      if (r == 0 && tid == 0) __hip_atomic_store((GAS u64*)(ws + WS_EPOCH), epoch, __ATOMIC_RELAXED, __HIP_MEMORY_SCOPE_AGENT);
      STAMP(10);
    }
    STAMP_FLUSH();
  }
}

extern "C" void kernel_launch(void* const* d_in, const int* in_sizes, int n_in, void* d_out, int out_size, void* d_ws, size_t ws_size,
                              hipStream_t stream) {
  fused_kernel<<<256, 512, 0, stream>>>((const u16*)d_in[0], (const u16*)d_in[1], (const u16*)d_in[2], (const u16*)d_in[3],
                                        (const u16*)d_in[4], (const u16*)d_in[5], (const u16*)d_in[7], (const u16*)d_in[8],
                                        (const u16*)d_in[9], (const u16*)d_in[10], (const u16*)d_in[11], (u16*)d_out, (char*)d_ws + WS_BASE);
}
```

```cpp
#include <hip/hip_runtime.h>
#include <hip/hip_bf16.h>
#include <stdint.h>

#define GAS __attribute__((address_space(1)))
#define LAS __attribute__((address_space(3)))
#define DI __device__ __forceinline__
typedef unsigned short u16;
typedef unsigned long long u64;
typedef unsigned v4u __attribute__((ext_vector_type(4)));
typedef unsigned v2u __attribute__((ext_vector_type(2)));
typedef float f32x2 __attribute__((ext_vector_type(2)));
typedef float f32x4 __attribute__((ext_vector_type(4)));
typedef float f32x16 __attribute__((ext_vector_type(16)));
typedef short bf16x8 __attribute__((ext_vector_type(8)));
typedef short s16x4 __attribute__((ext_vector_type(4)));
typedef __bf16 bf2_t __attribute__((ext_vector_type(2)));

constexpr int NM = 576, NH = 32, ND = 128, NC = 512, NSK = 8192, NF = 4608;
constexpr int WS_EPOCH = 0;
constexpr int WS_HX = 4096;
constexpr int WS_QX = WS_HX + 4 * 73728;
constexpr int WS_CX = WS_QX + 262144;
constexpr int WS_QABS = WS_CX + 262144;
constexpr int WS_STAT = WS_QABS + 131072;
constexpr int WS_POUT = WS_STAT + 524288;
constexpr int WS_OPART = WS_POUT + 72 * 128 * 128;
constexpr int WS_STAMP = WS_OPART + 4194304;
constexpr int WS_BYTES = WS_STAMP + 2 * 256 * 256;
constexpr int AUX_SC1 = 16;
constexpr unsigned SPIN_CAP = 1u << 17;

#define CKV_N1 0
#define CKV_WIN 2
#define CKV_WIN2 16
#define PRETOUCH 0
#define XPOL ""
#define STAT_SW 1
#define STAT_SLEEP 2
#define OAUX 16
#define OPRE 2
#define WS_BASE 0
#define BULK_WIN 32
#define PPRE 0
#define HO0_SLEEP 3
#define T_CKV 150
#define POLL_SLEEP 2
#define T_STATS 850
#define T_HEADB 470
#define T_RED 800

#define STAMP(i) do {} while (0)
#define STAMPW(wv, i) do {} while (0)
#define STAMP_FLUSH() do {} while (0)
#define CYC(i) do {} while (0)

DI unsigned fsw(unsigned r) { return ((r & 3u) << 2) | ((((r >> 3) ^ (r >> 2)) & 1u) << 1) | ((r >> 3) & 1u); }
DI float bflo(unsigned v) { return __uint_as_float(v << 16); }
DI float bfhi(unsigned v) { return __uint_as_float(v & 0xffff0000u); }
DI unsigned pk2(float a, float b) { f32x2 f = {a, b}; bf2_t r = __builtin_convertvector(f, bf2_t); return __builtin_bit_cast(unsigned, r); }
DI unsigned bf1(float a) { return pk2(a, 0.f) & 0xffffu; }
DI f32x4 mfma16(v4u a, v4u b, f32x4 c) { return __builtin_amdgcn_mfma_f32_16x16x32_bf16(__builtin_bit_cast(bf16x8, a), __builtin_bit_cast(bf16x8, b), c, 0, 0, 0); }
DI f32x16 mfma32(v4u a, v4u b, f32x16 c) { return __builtin_amdgcn_mfma_f32_32x32x16_bf16(__builtin_bit_cast(bf16x8, a), __builtin_bit_cast(bf16x8, b), c, 0, 0, 0); }
DI v2u trread(LAS char* p) { s16x4 r = __builtin_amdgcn_ds_read_tr16_b64_v4i16((LAS s16x4*)p); return __builtin_bit_cast(v2u, r); }
DI v4u trfrag(LAS char* p0, LAS char* p1) { v2u a = trread(p0), b = trread(p1); return v4u{a.x, a.y, b.x, b.y}; }
DI v4u lds128(LAS char* p) { return *(LAS v4u*)p; }
DI void dma16(const u16* g, LAS char* l) { __builtin_amdgcn_global_load_lds((const GAS unsigned*)g, (LAS unsigned*)l, 16, 0, 2); }
DI v4u gld(const u16* p) { return *(const GAS v4u*)p; }
DI v4u gldnt(const u16* p) { return __builtin_nontemporal_load((const GAS v4u*)p); }
DI v2u gldnt8(const u16* p) { return __builtin_nontemporal_load((const GAS v2u*)p); }
template <int CTRL> DI float dppf(float x) { return __builtin_bit_cast(float, __builtin_amdgcn_mov_dpp(__builtin_bit_cast(int, x), CTRL, 0xf, 0xf, true)); }
template <int CTRL> DI unsigned dppu(unsigned x) { return (unsigned)__builtin_amdgcn_mov_dpp((int)x, CTRL, 0xf, 0xf, true); }
DI float sum32(float x) { auto t = __builtin_amdgcn_permlane32_swap(__float_as_uint(x), __float_as_uint(x), false, false); return __uint_as_float(t[0]) + __uint_as_float(t[1]); }
DI float max32(float x) { auto t = __builtin_amdgcn_permlane32_swap(__float_as_uint(x), __float_as_uint(x), false, false); return fmaxf(__uint_as_float(t[0]), __uint_as_float(t[1])); }
DI float sum16(float x) { auto t = __builtin_amdgcn_permlane16_swap(__float_as_uint(x), __float_as_uint(x), false, false); return __uint_as_float(t[0]) + __uint_as_float(t[1]); }
DI float max16(float x) { auto t = __builtin_amdgcn_permlane16_swap(__float_as_uint(x), __float_as_uint(x), false, false); return fmaxf(__uint_as_float(t[0]), __uint_as_float(t[1])); }
DI float wave_sum(float x) { x += dppf<0xB1>(x); x += dppf<0x4E>(x); x += dppf<0x141>(x); x += dppf<0x128>(x); x = sum16(x); return sum32(x); }
DI float wave_max(float x) { x = fmaxf(x, dppf<0xB1>(x)); x = fmaxf(x, dppf<0x4E>(x)); x = fmaxf(x, dppf<0x141>(x)); x = fmaxf(x, dppf<0x128>(x)); x = max16(x); return max32(x); }
DI void wait_until(u64 t) { while (__builtin_amdgcn_s_memrealtime() < t) __builtin_amdgcn_s_sleep(1); }
#define DRAIN() asm volatile("s_waitcnt vmcnt(0)" ::: "memory")
#define CBAR() asm volatile("" ::: "memory")
#define SCHED() __builtin_amdgcn_sched_barrier(0)
#define LBAR() asm volatile("s_waitcnt lgkmcnt(0)\n\ts_barrier" ::: "memory")
#define DMA16(gp, ldsoff) do { unsigned keep_; asm volatile("s_mov_b32 %0, m0\n\ts_mov_b32 m0, %2\n\ts_nop 0\n\tglobal_load_lds_dwordx4 %1, off nt\n\ts_mov_b32 m0, %0" : "=&s"(keep_) : "v"(gp), "s"((unsigned)(size_t)L + (unsigned)(ldsoff)) : "memory"); } while (0)
#define TOUCH(dst, off) asm volatile("buffer_load_dword %0, %1, %2, 0 offen sc1" : "=&v"(dst) : "v"(off), "s"(rs) : "memory")
#define DMA16X(gp, ldsoff) do { unsigned keep_; asm volatile("s_mov_b32 %0, m0\n\ts_mov_b32 m0, %2\n\ts_nop 0\n\tglobal_load_lds_dwordx4 %1, off" XPOL "\n\ts_mov_b32 m0, %0" : "=&s"(keep_) : "v"(gp), "s"((unsigned)(size_t)L + (unsigned)(ldsoff)) : "memory"); } while (0)
#define DMA16W(gp, ldsoff) do { DMA16(gp, ldsoff); if (BULK_WIN < 32) asm volatile("s_waitcnt vmcnt(%0)" :: "i"(BULK_WIN - 1) : "memory"); } while (0)
#define LDG(off) __builtin_amdgcn_raw_buffer_load_b128(rs, (off), 0, AUX_SC1)
#define STG(v, off) __builtin_amdgcn_raw_buffer_store_b128((v), rs, (off), 0, AUX_SC1)
#define TAGOK(g) (((g).x == tagA) & ((g).z == tagB))

DI int tile_lane_off(int lane, int ld) { const int r8 = lane >> 3; return (8 * (r8 >> 2) + (r8 & 3)) * ld + 8 * ((lane & 7) ^ ((r8 >> 1) << 1)); }
DI int tile_tr_off(int lane, int ct) { const int r8 = 4 * ((lane >> 4) & 1) + ((lane >> 2) & 3); return (lane >> 5) * 1024 + r8 * 128 + ((ct ^ (r8 >> 1)) * 32) + (lane & 3) * 8; }
DI void put_hilo(LAS char* base, int stride, int d, float v0, float v1) {
  const unsigned h = pk2(v0, v1);
  const unsigned l = pk2(v0 - bflo(h), v1 - bfhi(h));
  *(LAS u16*)(base + d * 2) = (u16)h;
  *(LAS u16*)(base + stride + d * 2) = (u16)(h >> 16);
  *(LAS u16*)(base + 2 * stride + d * 2) = (u16)l;
  *(LAS u16*)(base + 3 * stride + d * 2) = (u16)(l >> 16);
}

__global__ void __launch_bounds__(512, 2)
fused_kernel(const u16* __restrict__ x, const u16* __restrict__ ckv, const u16* __restrict__ wq, const u16* __restrict__ wuk,
             const u16* __restrict__ wuv, const u16* __restrict__ wo, const u16* __restrict__ w1, const u16* __restrict__ b1,
             const u16* __restrict__ w2, const u16* __restrict__ b2, const u16* __restrict__ gw, u16* __restrict__ out, char* ws) {
  __shared__ __attribute__((aligned(16))) char smem[163840];
  LAS char* L = (LAS char*)smem;
  const int tid = threadIdx.x, lane = tid & 63, w = __builtin_amdgcn_readfirstlane(tid >> 6);
  const int bid = blockIdx.x, xcd = bid & 7, kk = bid >> 3;
  const u64 t0 = __builtin_amdgcn_s_memrealtime();
  const __amdgpu_buffer_rsrc_t rs = __builtin_amdgcn_make_buffer_rsrc((void*)ws, 0, WS_BYTES, 0x00020000);
#define LOAD_EPOCH() (__hip_atomic_load((GAS u64*)(ws + WS_EPOCH), __ATOMIC_RELAXED, __HIP_MEMORY_SCOPE_AGENT) + 1ull)
  STAMP(0);
  const int g4 = lane >> 4, col = lane & 15;

  if (kk < 16) {
    const int h = xcd * 4 + (kk >> 2), j = kk & 3;
    constexpr int WQ = 0, WUK = 40960, W1 = 69632, W2 = W1 + 40960, W2X = W2 + 36864, WUV = 0, WO = 32768;
    constexpr int RED = W2X + 9216, CTXA = RED + 4096, QA = CTXA + 1024, PRIV = RED, HR = QA + 512;
    const int ks0 = j < 2 ? 5 * j : 4 * j + 2;
    u64 epoch;
    asm volatile("global_load_dwordx2 %0, %1, off sc1" : "=&v"(epoch) : "v"(ws + WS_EPOCH) : "memory");
    constexpr int XG = W2X;
    if (w < 3) { const int c = 64 * w + lane; DMA16X(x + 8 * (c < 144 ? c : 0), XG + w * 1024); }
    else if (w < 5) { const int c = 64 * (w - 3) + lane; DMA16X(gw + 8 * (c < 72 ? c : 0), XG + w * 1024); }
    {
      const u16* src = wq + (size_t)(32 * ks0) * 4096 + h * ND + tile_lane_off(lane, 4096) + (16 * (w & 1) + 4 * (w >> 2)) * 4096 + 64 * ((w >> 1) & 1);
#pragma unroll
      for (int i = 0; i < 5; ++i)
        if (i < 4 || j < 2) DMA16(src + (size_t)i * 32 * 4096, WQ + (i * 8 + w) * 1024);
#pragma unroll
      for (int i = 0; i < 4; ++i) {
        const int row = 16 * w + 4 * i + g4;
        DMA16(wuk + (size_t)(128 * j + row) * 4096 + h * ND + 8 * (col ^ fsw(4 * i + g4)), WUK + (4 * w + i) * 1024);
      }
    }
    asm volatile("s_waitcnt vmcnt(4)\n\ts_barrier" : "+v"(epoch) :: "memory");
    v4u xa[5];
#pragma unroll
    for (int i = 0; i < 5; ++i) xa[i] = lds128(L + XG + (col & 1) * 1152 + 64 * (ks0 + ((i < 4 || j < 2) ? i : 0)) + 16 * g4);
    epoch += 1ull;
    if ((unsigned)epoch == 0u) epoch += 1ull;
    const unsigned tagA = (unsigned)epoch, tagB = (unsigned)(epoch >> 32);
    STAMP(1);
    {
      f32x4 acc = {0.f, 0.f, 0.f, 0.f};
      LAS char* p = L + WQ + (w >> 2) * 2048 + tile_tr_off(lane, w & 3);
      v4u bq[5];
#pragma unroll
      for (int i = 0; i < 4; ++i) bq[i] = trfrag(p + i * 8192, p + i * 8192 + 4096);
      if (j < 2) bq[4] = trfrag(p + 4 * 8192, p + 4 * 8192 + 4096);
#pragma unroll
      for (int i = 0; i < 4; ++i) acc = mfma16(xa[i], bq[i], acc);
      if (j < 2) acc = mfma16(xa[4], bq[4], acc);
      if (g4 == 0) { const v4u gr = {tagA, __float_as_uint(acc[0]), tagB, __float_as_uint(acc[1])}; STG(gr, WS_QX + ((h * 4 + j) * 128 + 16 * w + col) * 16); }
    }
    STAMP(2);
    float g0, g1;
    {
      const int gc = lane + 64 < 72 ? lane + 64 : 0;
      const v4u gq[6] = {lds128(L + XG + lane * 16), lds128(L + XG + 1152 + lane * 16), lds128(L + XG + 3072 + lane * 16),
                         lds128(L + XG + gc * 16), lds128(L + XG + 1152 + gc * 16), lds128(L + XG + 3072 + gc * 16)};
      float d0 = 0.f, d1 = 0.f, e0 = 0.f, e1 = 0.f;
#pragma unroll
      for (int e = 0; e < 4; ++e) {
        d0 += bflo(gq[0][e]) * bflo(gq[2][e]) + bfhi(gq[0][e]) * bfhi(gq[2][e]);
        d1 += bflo(gq[1][e]) * bflo(gq[2][e]) + bfhi(gq[1][e]) * bfhi(gq[2][e]);
        e0 += bflo(gq[3][e]) * bflo(gq[5][e]) + bfhi(gq[3][e]) * bfhi(gq[5][e]);
        e1 += bflo(gq[4][e]) * bflo(gq[5][e]) + bfhi(gq[4][e]) * bfhi(gq[5][e]);
      }
      if (lane < 8) { d0 += e0; d1 += e1; }
      d0 = wave_sum(d0); d1 = wave_sum(d1);
      g0 = 1.f / (1.f + __expf(-d0)); g1 = 1.f / (1.f + __expf(-d1));
      asm volatile("" : "+v"(g0), "+v"(g1));
    }
    DRAIN();
    __builtin_amdgcn_s_sleep(HO0_SLEEP);
    LAS char* pv = L + PRIV + w * 256;
    {
      v4u ga;
      const int off = WS_QX + ((h * 4 + g4) * 128 + 16 * w + col) * 16;
      for (unsigned spins = 0;; ++spins) {
        CBAR();
        ga = LDG(off);
        if (__all(TAGOK(ga)) || spins > SPIN_CAP) break;
        __builtin_amdgcn_s_sleep(1);
      }
      STAMP(3);
      v4u uk[4];
#pragma unroll
      for (int ks = 0; ks < 4; ++ks) uk[ks] = lds128(L + WUK + (16 * w + col) * 256 + (((4 * ks + g4) ^ fsw(col)) << 4));
      const float q0 = sum32(sum16(__uint_as_float(ga.y))), q1 = sum32(sum16(__uint_as_float(ga.w)));
      const float qv = (g4 & 1) ? q1 : q0;
      const unsigned hi = bf1(qv);
      *(LAS u16*)(L + QA + g4 * 256 + (16 * w + col) * 2) = (u16)((g4 & 2) ? bf1(qv - bflo(hi)) : hi);
      LBAR();
      f32x4 a = {0.f, 0.f, 0.f, 0.f};
#pragma unroll
      for (int ks = 0; ks < 4; ++ks) a = mfma16(lds128(L + QA + (col & 3) * 256 + ks * 64 + g4 * 16), uk[ks], a);
      const float v = ((g4 & 1) ? (a[1] + a[3]) : (a[0] + a[2])) * (0.08838834764831845f * 1.4426950408889634f);
      const unsigned bits = bf1(v);
      const unsigned n1 = dppu<0x55>(bits), n2 = dppu<0xAA>(bits), n3 = dppu<0xFF>(bits);
      if ((lane & 3) == 0 && g4 < 2) {
        const v4u gr = {tagA, bits | (n1 << 16), tagB, n2 | (n3 << 16)};
        STG(gr, WS_QABS + ((g4 * 32 + h) * 128 + 32 * j + 4 * w + (col >> 2)) * 16);
      }
    }
    STAMP(4);
    const int pidx = h * 4 + j;
    const bool dbl = kk >= 14;
    const int u0 = (xcd & 1) ? (dbl ? 12 + 4 * (kk - 14) : kk < 12 ? kk : kk + 2) : (dbl ? 10 + 4 * (kk - 14) : kk < 10 ? kk : kk < 12 ? kk + 2 : kk + 4);
    const int r2 = dbl ? 4480 + 8 * (xcd * 2 + kk - 14) : 40 * (xcd * 14 + kk);
    const int nk2 = dbl ? 8 : 40;
    unsigned b1v[3];
    {
      const int ug = xcd * 18 + u0, uq = ug & 3, f0 = 128 * (ug >> 2);
      const int kq0 = uq < 2 ? 5 * uq : 4 * uq + 2, kq1 = 14;
      v4u xm[2][5];
#pragma unroll
      for (int i = 0; i < 5; ++i) {
        const int ki = (i < 4 || uq < 2) ? i : 0;
        xm[0][i] = lds128(L + XG + (col & 1) * 1152 + 64 * (kq0 + ki) + 16 * g4);
        xm[1][i] = lds128(L + XG + (col & 1) * 1152 + 64 * (kq1 + ki) + 16 * g4);
      }
      LBAR();
      if (T_HEADB > 0) wait_until(t0 + T_HEADB);
      {
        const u16* src = wuv + (size_t)(128 * j) * 4096 + h * ND + tile_lane_off(lane, 4096) + (16 * (w & 1) + 4 * (w >> 2)) * 4096 + 64 * ((w >> 1) & 1);
#pragma unroll
        for (int i = 0; i < 4; ++i) DMA16W(src + (size_t)i * 32 * 4096, WUV + (i * 8 + w) * 1024);
        const u16* so = wo + (size_t)(h * ND + 32 * j) * NM + tile_lane_off(lane, NM) + (16 * (w & 1) + 4 * ((w >> 1) & 1)) * NM + 64 * (w >> 2);
#pragma unroll
        for (int i = 0; i < 5; ++i)
          if (i < 4 || w < 4) DMA16W(so + 128 * i, WO + (i * 8 + w) * 1024);
      }
      {
        const int r8 = lane >> 3;
        const u16* s3 = w2 + (size_t)(r2 + nk2 - 8 + r8) * NM + 8 * ((lane & 7) ^ ((r8 >> 1) << 1));
        DMA16W(s3 + 64 * w, W2X + w * 1024);
        if (w == 0) DMA16W(s3 + 512, W2X + 8192);
      }
      if (!dbl) {
        const u16* s2 = w2 + (size_t)r2 * NM + tile_lane_off(lane, NM) + (16 * (w & 1) + 4 * ((w >> 1) & 1)) * NM + 64 * (w >> 2);
#pragma unroll
        for (int i = 0; i < 5; ++i)
          if (i < 4 || w < 4) DMA16W(s2 + 128 * i, W2 + (i * 8 + w) * 1024);
      }
      {
        const int fb = r2 + col;
        asm volatile("global_load_ushort %0, %3, off\n\tglobal_load_ushort %1, %4, off\n\tglobal_load_ushort %2, %5, off"
                     : "=&v"(b1v[0]), "=&v"(b1v[1]), "=&v"(b1v[2])
                     : "v"(b1 + (col < nk2 ? fb : r2)), "v"(b1 + (16 + col < nk2 ? fb + 16 : r2)), "v"(b1 + (32 + col < nk2 ? fb + 32 : r2)) : "memory");
      }
      {
        const u16* s1 = w1 + f0 + tile_lane_off(lane, NF) + (size_t)(16 * (w & 1) + 4 * (w >> 2)) * NF + 64 * ((w >> 1) & 1);
#pragma unroll
        for (int i = 0; i < 5; ++i)
          if (i < 4 || uq < 2) DMA16W(s1 + (size_t)(kq0 + i) * 32 * NF, W1 + (i * 8 + w) * 1024);
        if (dbl) {
#pragma unroll
          for (int i = 0; i < 4; ++i) DMA16W(s1 + (size_t)(kq1 + i) * 32 * NF, W2 + (i * 8 + w) * 1024);
        }
      }
      asm volatile("s_waitcnt vmcnt(0)\n\ts_barrier" : "+v"(b1v[0]), "+v"(b1v[1]), "+v"(b1v[2]) :: "memory");
      STAMP(10);
#pragma unroll
      for (int un = 0; un < 2; ++un) {
        if (un == 0 || dbl) {
          LAS char* p = L + (un == 0 ? W1 : W2) + (w >> 2) * 2048 + tile_tr_off(lane, w & 3);
          f32x4 acc = {0.f, 0.f, 0.f, 0.f};
#pragma unroll
          for (int i = 0; i < 4; ++i) acc = mfma16(xm[un][i], trfrag(p + i * 8192, p + i * 8192 + 4096), acc);
          if (uq < 2) acc = mfma16(xm[un][4], trfrag(p + 4 * 8192, p + 4 * 8192 + 4096), acc);
          if (g4 == 0) {
            const v4u gr = {tagA, __float_as_uint(acc[0]), tagB, __float_as_uint(acc[1])};
            STG(gr, WS_HX + ((uq + un) * NF + f0 + 16 * w + col) * 16);
          }
        }
      }
      STAMP(11);
    }
    v4u uv[4], wf[5];
    {
      LAS char* p = L + WUV + (w >> 2) * 2048 + tile_tr_off(lane, w & 3);
#pragma unroll
      for (int i = 0; i < 4; ++i) uv[i] = trfrag(p + i * 8192, p + i * 8192 + 4096);
      LAS char* po = L + WO + (w >> 2) * 4096 + tile_tr_off(lane, w & 3);
#pragma unroll
      for (int i = 0; i < 5; ++i) wf[i] = (i < 4 || w < 4) ? trfrag(po + i * 8192, po + i * 8192 + 2048) : v4u{0u, 0u, 0u, 0u};
    }
    v4u w2m[5], w2x[5];
    {
      const int q = (lane >> 2) & 3;
      LAS char* p = L + W2X + (w >> 2) * 1024 + q * 128 + (lane & 3) * 8;
      const int o0 = (((w & 3) ^ (q >> 1)) * 32), o1 = 512 + (((w & 3) ^ (2 + (q >> 1))) * 32);
      LAS char* pm = L + W2 + (w >> 2) * 4096 + tile_tr_off(lane, w & 3);
#pragma unroll
      for (int i = 0; i < 5; ++i) {
        const int ii = (i < 4 || w < 4) ? i : 0;
        w2x[i] = trfrag(p + ii * 2048 + o0, p + ii * 2048 + o1);
        w2m[i] = trfrag(pm + ii * 8192, pm + ii * 8192 + 2048);
        if (dbl) w2m[i] = v4u{0u, 0u, 0u, 0u};
      }
    }
    STAMP(5);
    const int b = w >> 2, q4 = w & 3;
    float wgt;
    v4u hg = {0u, 0u, 0u, 0u}, cxw = {0u, 0u, 0u, 0u};
    const int ht = w - 2, hf_ = 16 * ht + col;
    const int ho = WS_HX + (g4 * NF + r2 + (hf_ < nk2 ? hf_ : 0)) * 16;
    constexpr int HA = QA;
    {
      const int off = WS_STAT + (((b * 64 + 16 * q4 + ((lane >> 1) & 15)) * 8 + 2 * j + (lane & 1)) * 32 + h) * 16;
      const int ooff = WS_OPART + ((((h * 4 + j) * 2 + b) * 64 + 16 * q4) * 256) + lane * 16;
      v4u g, o[4];
      bool ok = lane >= 32;
      unsigned done = 0;
      if (w < 2) { cxw = LDG(WS_CX + ((h * 4 + j) * 128 + w * 64 + lane) * 16); asm volatile("" :: "v"(cxw)); }
      wait_until(t0 + T_STATS);
      for (unsigned spins = 0;; ++spins) {
        CBAR();
        if (!ok) { g = LDG(off); ok = TAGOK(g); }
        const unsigned m = (unsigned)__builtin_amdgcn_ballot_w64(ok) | (spins > SPIN_CAP ? 0xffffffffu : 0u);
        __builtin_amdgcn_fence(__ATOMIC_ACQUIRE, "wavefront");
#pragma unroll
        for (int i = 0; i < 4; ++i)
          if (!(done & (1u << i)) && ((m >> (8 * i)) & 0xffu) == 0xffu) { o[i] = LDG(ooff + i * 1024); done |= 1u << i; }
        if (done == 15u) break;
        __builtin_amdgcn_s_sleep(STAT_SLEEP);
      }
      if (ht >= 0 && ht < 3) hg = LDG(ho);
      STAMPW(0, 6);
      const float ms = lane < 32 ? __uint_as_float(g.y) : -INFINITY, ls = (lane < 32 && !(lane & 1)) ? __uint_as_float(g.w) : 0.f;
      float Mw = fmaxf(ms, dppf<0x4E>(ms));
      Mw = fmaxf(Mw, dppf<0x141>(Mw)); Mw = fmaxf(Mw, dppf<0x128>(Mw)); Mw = max32(max16(Mw));
      wgt = __builtin_amdgcn_exp2f(ms - Mw);
      float ww[4];
#pragma unroll
      for (int i = 0; i < 4; ++i) ww[i] = __uint_as_float((unsigned)__builtin_amdgcn_ds_bpermute((4 * i + g4) * 8, (int)__float_as_uint(wgt)));
      float Lw = wgt * ls;
      Lw += dppf<0xB1>(Lw); Lw += dppf<0x4E>(Lw); Lw += dppf<0x141>(Lw); Lw += dppf<0x128>(Lw); Lw = sum32(sum16(Lw));
      if (lane == 0) *(LAS f32x2*)(L + HR + w * 8) = f32x2{Mw, Lw};
      asm volatile("" : "+v"(ww[0]), "+v"(ww[1]), "+v"(ww[2]), "+v"(ww[3]));
      SCHED();
      float acc[8] = {0.f, 0.f, 0.f, 0.f, 0.f, 0.f, 0.f, 0.f};
#pragma unroll
      for (int i = 0; i < 4; ++i) {
        acc[0] += ww[i] * bflo(o[i].x); acc[1] += ww[i] * bfhi(o[i].x);
        acc[2] += ww[i] * bflo(o[i].y); acc[3] += ww[i] * bfhi(o[i].y);
        acc[4] += ww[i] * bflo(o[i].z); acc[5] += ww[i] * bfhi(o[i].z);
        acc[6] += ww[i] * bflo(o[i].w); acc[7] += ww[i] * bfhi(o[i].w);
      }
      float r4[4];
#pragma unroll
      for (int e = 0; e < 4; ++e) {
        auto t = __builtin_amdgcn_permlane32_swap(__float_as_uint(acc[e]), __float_as_uint(acc[4 + e]), false, false);
        r4[e] = sum16(__uint_as_float(t[0]) + __uint_as_float(t[1]));
      }
      if ((g4 & 1) == 0) *(LAS f32x4*)(L + RED + w * 512 + col * 32 + (lane >> 5) * 16) = f32x4{r4[0], r4[1], r4[2], r4[3]};
    }
    LBAR();
    if (tid < 128) {
      LAS float* r = (LAS float*)(L + RED) + tid;
      const f32x4 s0 = *(LAS f32x4*)(L + HR), s1 = *(LAS f32x4*)(L + HR + 16), s2 = *(LAS f32x4*)(L + HR + 32), s3 = *(LAS f32x4*)(L + HR + 48);
      const float Ma = fmaxf(fmaxf(s0[0], s0[2]), fmaxf(s1[0], s1[2])), Mb = fmaxf(fmaxf(s2[0], s2[2]), fmaxf(s3[0], s3[2]));
      const float a0 = __builtin_amdgcn_exp2f(s0[0] - Ma), a1 = __builtin_amdgcn_exp2f(s0[2] - Ma), a2 = __builtin_amdgcn_exp2f(s1[0] - Ma), a3 = __builtin_amdgcn_exp2f(s1[2] - Ma);
      const float c0 = __builtin_amdgcn_exp2f(s2[0] - Mb), c1 = __builtin_amdgcn_exp2f(s2[2] - Mb), c2 = __builtin_amdgcn_exp2f(s3[0] - Mb), c3 = __builtin_amdgcn_exp2f(s3[2] - Mb);
      const float ia = __builtin_amdgcn_rcpf((a0 * s0[1] + a1 * s0[3]) + (a2 * s1[1] + a3 * s1[3]));
      const float ib = __builtin_amdgcn_rcpf((c0 * s2[1] + c1 * s2[3]) + (c2 * s3[1] + c3 * s3[3]));
      put_hilo(L + CTXA, 256, tid, ((a0 * r[0] + a1 * r[128]) + (a2 * r[256] + a3 * r[384])) * ia, ((c0 * r[512] + c1 * r[640]) + (c2 * r[768] + c3 * r[896])) * ib);
    } else if (w < 5) {
      for (unsigned spins = 0; !__all(TAGOK(hg)) && spins < SPIN_CAP; ++spins) {
        __builtin_amdgcn_s_sleep(POLL_SLEEP);
        CBAR();
        hg = LDG(ho);
      }
      const float bb = bflo(ht == 0 ? b1v[0] : ht == 1 ? b1v[1] : b1v[2]);
      const float p0 = fmaxf(sum32(sum16(__uint_as_float(hg.y))) + bb, 0.f), p1 = fmaxf(sum32(sum16(__uint_as_float(hg.w))) + bb, 0.f);
      const float hv = hf_ < nk2 ? ((g4 & 1) ? p1 * p1 : p0 * p0) : 0.f;
      const unsigned hi = bf1(hv);
      const int kpos = dbl ? 32 - 16 * ht + col : hf_;
      *(LAS u16*)(L + HA + g4 * 128 + kpos * 2) = (u16)((g4 & 2) ? bf1(hv - bflo(hi)) : hi);
    } else if (w == 5) {
      *(LAS u16*)(L + HA + g4 * 128 + (48 + col) * 2) = 0;
    }
    LBAR();
    {
      f32x4 acc = {0.f, 0.f, 0.f, 0.f};
#pragma unroll
      for (int i = 0; i < 4; ++i) acc = mfma16(lds128(L + CTXA + (col & 3) * 256 + i * 64 + g4 * 16), uv[i], acc);
      if (g4 == 0) {
        const v4u gr = {tagA, __float_as_uint(acc[0] + acc[2]), tagB, __float_as_uint(acc[1] + acc[3])};
        STG(gr, WS_CX + (((h * 4 + (w >> 1)) * 4 + j) * 32 + 16 * (w & 1) + col) * 16);
      }
    }
    STAMP(7);
    f32x2 mpa, mpb;
    {
      const v4u ha0 = lds128(L + HA + (col & 3) * 128 + g4 * 16), hax = lds128(L + HA + (col & 3) * 128 + 64 + g4 * 16);
      f32x4 acc[5];
#pragma unroll
      for (int i = 0; i < 5; ++i) acc[i] = mfma16(ha0, w2m[i], mfma16(hax, w2x[i], f32x4{0.f, 0.f, 0.f, 0.f}));
      const f32x4 a = g4 == 0 ? acc[0] : g4 == 1 ? acc[1] : g4 == 2 ? acc[2] : acc[3];
      mpa = f32x2{(1.f - g0) * (a[0] + a[2]), (1.f - g1) * (a[1] + a[3])};
      mpb = f32x2{(1.f - g0) * (acc[4][0] + acc[4][2]), (1.f - g1) * (acc[4][1] + acc[4][3])};
    }
    {
      v4u ga, gb;
      const int off = WS_CX + ((h * 4 + j) * 128 + lane) * 16;
      for (unsigned spins = 0;; ++spins) {
        CBAR();
        ga = LDG(off); gb = LDG(off + 1024);
        if (__all(TAGOK(ga) & TAGOK(gb)) || spins > SPIN_CAP) break;
        __builtin_amdgcn_s_sleep(1);
      }
      STAMP(8);
      const float v0 = sum32(__uint_as_float(ga.y) + __uint_as_float(gb.y)), v1 = sum32(__uint_as_float(ga.w) + __uint_as_float(gb.w));
      if (lane < 32) put_hilo(pv, 64, lane, v0, v1);
      const v4u ca = lds128(pv + (col & 3) * 64 + g4 * 16);
      f32x4 acc[5];
#pragma unroll
      for (int i = 0; i < 5; ++i) acc[i] = mfma16(ca, wf[i], f32x4{0.f, 0.f, 0.f, 0.f});
      const f32x4 a = g4 == 0 ? acc[0] : g4 == 1 ? acc[1] : g4 == 2 ? acc[2] : acc[3];
      {
        const int ct = w + 8 * g4;
        const f32x2 mp = mpa;
        const v4u gr = {tagA, __float_as_uint(g0 * (a[0] + a[2]) + mp.x), tagB, __float_as_uint(g1 * (a[1] + a[3]) + mp.y)};
        STG(gr, WS_POUT + ((2 * ct + (col >> 3)) * 128 + pidx) * 128 + (col & 7) * 16);
      }
      if (w < 4 && g4 == 0) {
        const int ct = w + 32;
        const f32x2 mp = mpb;
        const v4u gr = {tagA, __float_as_uint(g0 * (acc[4][0] + acc[4][2]) + mp.x), tagB, __float_as_uint(g1 * (acc[4][1] + acc[4][3]) + mp.y)};
        STG(gr, WS_POUT + ((2 * ct + (col >> 3)) * 128 + pidx) * 128 + (col & 7) * 16);
      }
    }
    STAMP(9);
    STAMP_FLUSH();
    __builtin_amdgcn_endpgm();
  } else {
    const int a = xcd * 16 + (kk - 16), b = a >> 6, s = a & 63;
    constexpr int QO = 131072, XCH = 32768, PB = XCH + 16384, MX = XCH + 24576, LS = XCH + 25600;
    u64 epoch;
    asm volatile("global_load_dwordx2 %0, %1, off sc1" : "=&v"(epoch) : "v"(ws + WS_EPOCH) : "memory");
    {
      const u16* src = ckv + ((size_t)b * NSK + 128 * s + 16 * w) * NC;
#pragma unroll
      for (int i = 0; i < 16; ++i) {
        if (i == CKV_N1) wait_until(t0 + T_CKV);
        DMA16(src + i * NC + ((lane ^ fsw(i)) * 8), (16 * w + i) * 1024);
        if (i < CKV_N1 && i >= CKV_WIN - 1) asm volatile("s_waitcnt vmcnt(%0)" :: "i"(CKV_WIN - 1) : "memory");
        if (i >= CKV_N1 && i < 15 && CKV_WIN2 < 16) asm volatile("s_waitcnt vmcnt(%0)" :: "i"(CKV_WIN2 - 1) : "memory");
      }
    }
    const int r32 = lane & 31, hf = lane >> 5, kt = w & 3, ch = w >> 2;
    const unsigned fr = fsw(r32 & 15);
    const unsigned ob = r32 * 1024 + ch * 512 + ((hf ^ fr) << 4);
    asm volatile("s_waitcnt vmcnt(0)\n\ts_barrier" : "+v"(epoch) :: "memory");
    epoch += 1ull;
    if ((unsigned)epoch == 0u) epoch += 1ull;
    const unsigned tagA = (unsigned)epoch, tagB = (unsigned)(epoch >> 32);
    STAMP(1);
    v4u kf[16], vf[8][2];
    {
      const int gl = (w & 1) * 64 + lane, hw = w >> 1;
      const int off = WS_QABS + ((b * 32 + hw) * 128 + gl) * 16;
      v4u g[8];
#pragma unroll
      for (int u = 0; u < 8; ++u) g[u] = LDG(off + u * 8192);
      SCHED();
#pragma unroll
      for (int st = 0; st < 16; ++st) kf[st] = lds128(L + kt * 32768 + ((ob ^ ((st & 7) << 5)) + (st >> 3) * 256));
      const int blk = (lane >> 4) & 1, q = (lane >> 2) & 3, p = lane & 3;
      unsigned va[2][2];
#pragma unroll
      for (int t = 0; t < 2; ++t)
#pragma unroll
        for (int cc = 0; cc < 2; ++cc)
          va[t][cc] = (8 * hf + 4 * t + q) * 1024 + (((8 * w + 4 * cc + 2 * blk + (p >> 1)) ^ (4 * q + 2 * (hf ^ t) + hf)) << 4) + 8 * (p & 1);
#pragma unroll
      for (int ks = 0; ks < 8; ++ks)
#pragma unroll
        for (int cc = 0; cc < 2; ++cc) vf[ks][cc] = trfrag(L + va[0][cc] + ks * 16384, L + va[1][cc] + ks * 16384);
      SCHED();
      unsigned todo = 0;
#pragma unroll
      for (int u = 0; u < 8; ++u) if (!__all(TAGOK(g[u]))) todo |= 1u << u;
      for (unsigned spins = 0; todo != 0 && spins < SPIN_CAP; ++spins) {
        __builtin_amdgcn_s_sleep(POLL_SLEEP);
        CBAR();
#pragma unroll
        for (int u = 0; u < 8; ++u) if (todo & (1u << u)) g[u] = LDG(off + u * 8192);
#pragma unroll
        for (int u = 0; u < 8; ++u) if (todo & (1u << u)) { if (__all(TAGOK(g[u]))) todo &= ~(1u << u); }
      }
      const unsigned qb = QO + hw * 1024 + (((gl >> 1) ^ (hw << 2)) << 4) + (gl & 1) * 8;
#pragma unroll
      for (int u = 0; u < 8; ++u) *(LAS v2u*)(L + ((qb ^ (fsw((4 * u) & 15) << 4)) + u * 4096)) = v2u{g[u].y, g[u].w};
      STAMP(2);
    }
    {
      const int go = WS_OPART + (((((lane >> 3) * 4 + (w >> 1)) * 2 + b) * 64 + s) * 256) + (w & 1) * 128 + ((lane & 7) << 4);
      const v4u z = {0u, 0u, 0u, 0u};
#pragma unroll
      for (int i = 0; i < 4; ++i) STG(z, go + i * (8 * 4 * 2 * 64 * 256));
      if (lane < 32) STG(z, WS_STAT + (((b * 64 + s) * 8 + w) * 32 + lane) * 16);
    }
    LBAR();
    STAMP(3);
    CYC(0);
    f32x16 S;
#pragma unroll
    for (int i = 0; i < 16; ++i) S[i] = 0.f;
    {
      v4u qf[16];
#pragma unroll
      for (int st = 0; st < 16; ++st) qf[st] = lds128(L + QO + ((ob ^ ((st & 7) << 5)) + (st >> 3) * 256));
      SCHED();
#pragma unroll
      for (int st = 0; st < 16; ++st) S = mfma32(kf[st], qf[st], S);
    }
    CYC(1);
    CYC(2);
    float sv[8];
    if (ch == 0) {
      LAS f32x4* xw = (LAS f32x4*)(L + XCH + w * 2048 + lane * 16);
      xw[0] = f32x4{S[8], S[9], S[10], S[11]}; xw[64] = f32x4{S[12], S[13], S[14], S[15]};
#pragma unroll
      for (int e = 0; e < 8; ++e) sv[e] = S[e];
    } else {
      LAS f32x4* xw = (LAS f32x4*)(L + XCH + w * 2048 + lane * 16);
      xw[0] = f32x4{S[0], S[1], S[2], S[3]}; xw[64] = f32x4{S[4], S[5], S[6], S[7]};
#pragma unroll
      for (int e = 0; e < 8; ++e) sv[e] = S[8 + e];
    }
    LBAR();
    CYC(3);
    {
      LAS f32x4* xr = (LAS f32x4*)(L + XCH + (w ^ 4) * 2048 + lane * 16);
      const f32x4 p0 = xr[0], p1 = xr[64];
#pragma unroll
      for (int e = 0; e < 4; ++e) { sv[e] += p0[e]; sv[4 + e] += p1[e]; }
      float m = fmaxf(fmaxf(fmaxf(sv[0], sv[1]), fmaxf(sv[2], sv[3])), fmaxf(fmaxf(sv[4], sv[5]), fmaxf(sv[6], sv[7])));
      m = max32(m);
      *(LAS float*)(L + MX + r32 * 32 + w * 4) = m;
    }
    LBAR();
    CYC(4);
    float Mx;
    {
      const f32x4 m0 = *(LAS f32x4*)(L + MX + r32 * 32), m1 = *(LAS f32x4*)(L + MX + r32 * 32 + 16);
      Mx = fmaxf(fmaxf(fmaxf(m0[0], m0[1]), fmaxf(m0[2], m0[3])), fmaxf(fmaxf(m1[0], m1[1]), fmaxf(m1[2], m1[3])));
      float l = 0.f;
#pragma unroll
      for (int e = 0; e < 8; ++e) { sv[e] = __builtin_amdgcn_exp2f(sv[e] - Mx); l += sv[e]; }
#pragma unroll
      for (int i = 0; i < 2; ++i)
        *(LAS v2u*)(L + PB + r32 * 256 + (((4 * kt + 2 * ch + i) ^ fr) << 4) + hf * 8) = v2u{pk2(sv[4 * i], sv[4 * i + 1]), pk2(sv[4 * i + 2], sv[4 * i + 3])};
      l = sum32(l);
      *(LAS float*)(L + LS + r32 * 32 + w * 4) = l;
    }
    LBAR();
    CYC(5);
    STAMP(4);
    float Ls;
    f32x16 O0, O1;
#pragma unroll
    for (int i = 0; i < 16; ++i) { O0[i] = 0.f; O1[i] = 0.f; }
    {
      const unsigned pb = PB + r32 * 256 + ((hf ^ fr) << 4);
      v4u pf[8];
#pragma unroll
      for (int ks = 0; ks < 8; ++ks) pf[ks] = lds128(L + (pb ^ (ks << 5)));
      const f32x4 l0 = *(LAS f32x4*)(L + LS + r32 * 32), l1 = *(LAS f32x4*)(L + LS + r32 * 32 + 16);
      SCHED();
#pragma unroll
      for (int ks = 0; ks < 8; ++ks) {
        O0 = mfma32(vf[ks][0], pf[ks], O0);
        O1 = mfma32(vf[ks][1], pf[ks], O1);
      }
      Ls = ((l0[0] + l0[1]) + (l0[2] + l0[3])) + ((l1[0] + l1[1]) + (l1[2] + l1[3]));
    }
    CYC(6);
    {
      LAS char* ot = L + w * 4096;
      const unsigned ow = r32 * 128 + 8 * hf;
#pragma unroll
      for (int gq = 0; gq < 4; ++gq) {
        *(LAS v2u*)(ot + ow + ((gq ^ (r32 & 7)) << 4)) = v2u{pk2(O0[4 * gq], O0[4 * gq + 1]), pk2(O0[4 * gq + 2], O0[4 * gq + 3])};
        *(LAS v2u*)(ot + ow + (((4 + gq) ^ (r32 & 7)) << 4)) = v2u{pk2(O1[4 * gq], O1[4 * gq + 1]), pk2(O1[4 * gq + 2], O1[4 * gq + 3])};
      }
      CYC(7);
      STAMP(5);
      const int go = WS_OPART + (((((lane >> 3) * 4 + (w >> 1)) * 2 + b) * 64 + s) * 256) + (w & 1) * 128 + (((lane & 7) ^ ((lane >> 3) & 7)) << 4);
#pragma unroll
      for (int i = 0; i < 4; ++i) __builtin_amdgcn_raw_buffer_store_b128(lds128(ot + i * 1024 + lane * 16), rs, go + i * (8 * 4 * 2 * 64 * 256), 0, OAUX);
    }
    SCHED();
    CYC(8);
    DRAIN();
    CYC(9);
    if (lane < 32) {
      const v4u gr = {tagA, __float_as_uint(Mx), tagB, __float_as_uint(Ls)};
      STG(gr, WS_STAT + (((b * 64 + s) * 8 + w) * 32 + lane) * 16);
    }
    CYC(10);
    STAMP(6);
    if ((a & 15) < 9) {
      const int r = (a >> 4) * 9 + (a & 15), t2 = tid - 256;
      float g0 = 0.f, g1 = 0.f, bias2 = 0.f;
      if (w == 4) {
        bias2 = bflo((unsigned)b2[8 * r + (lane & 7)]);
        float d0 = 0.f, d1 = 0.f;
#pragma unroll
        for (int i = 0; i < 2; ++i) {
          const int c = lane + 64 * i;
          if (c < 72) {
            const v4u xa = gld(x + c * 8), xb = gld(x + NM + c * 8), gg = gld(gw + c * 8);
#pragma unroll
            for (int e = 0; e < 4; ++e) {
              d0 += bflo(xa[e]) * bflo(gg[e]) + bfhi(xa[e]) * bfhi(gg[e]);
              d1 += bflo(xb[e]) * bflo(gg[e]) + bfhi(xb[e]) * bfhi(gg[e]);
            }
          }
        }
        d0 = wave_sum(d0); d1 = wave_sum(d1);
        g0 = 1.f / (1.f + __expf(-d0)); g1 = 1.f / (1.f + __expf(-d1));
      }
      const int off = WS_POUT + r * 16384 + tid * 16;
      v4u ga[2];
      wait_until(t0 + T_RED);
      for (unsigned spins = 0;; ++spins) {
        CBAR();
#pragma unroll
        for (int i = 0; i < 2; ++i) ga[i] = LDG(off + i * 8192);
        if (__all(TAGOK(ga[0]) & TAGOK(ga[1])) || spins > SPIN_CAP) break;
        __builtin_amdgcn_s_sleep(POLL_SLEEP);
      }
      STAMP(9);
      float v0 = __uint_as_float(ga[0].y) + __uint_as_float(ga[1].y);
      float v1 = __uint_as_float(ga[0].w) + __uint_as_float(ga[1].w);
      v0 += dppf<0x128>(v0); v1 += dppf<0x128>(v1);
      v0 = sum32(sum16(v0)); v1 = sum32(sum16(v1));
      if (lane < 8) { *(LAS float*)(L + 65536 + (lane * 8 + w) * 4) = v0; *(LAS float*)(L + 65536 + 256 + (lane * 8 + w) * 4) = v1; }
      LBAR();
      if (t2 >= 0 && t2 < 16) {
        const f32x4 p0 = *(LAS f32x4*)(L + 65536 + t2 * 32), p1 = *(LAS f32x4*)(L + 65536 + t2 * 32 + 16);
        const float v = ((p0[0] + p0[1]) + (p0[2] + p0[3])) + ((p1[0] + p1[1]) + (p1[2] + p1[3])) + (1.f - (t2 < 8 ? g0 : g1)) * bias2;
        out[(t2 >> 3) * NM + 8 * r + (t2 & 7)] = (u16)bf1(v);
      }
      if (r == 0 && tid == 0) __hip_atomic_store((GAS u64*)(ws + WS_EPOCH), epoch, __ATOMIC_RELAXED, __HIP_MEMORY_SCOPE_AGENT);
      STAMP(10);
    }
    STAMP_FLUSH();
  }
}

extern "C" void kernel_launch(void* const* d_in, const int* in_sizes, int n_in, void* d_out, int out_size, void* d_ws, size_t ws_size,
                              hipStream_t stream) {
  fused_kernel<<<256, 512, 0, stream>>>((const u16*)d_in[0], (const u16*)d_in[1], (const u16*)d_in[2], (const u16*)d_in[3],
                                        (const u16*)d_in[4], (const u16*)d_in[5], (const u16*)d_in[7], (const u16*)d_in[8],
                                        (const u16*)d_in[9], (const u16*)d_in[10], (const u16*)d_in[11], (u16*)d_out, (char*)d_ws + WS_BASE);
}
```

```cpp
#include <hip/hip_runtime.h>
#include <hip/hip_bf16.h>
#include <stdint.h>

#define GAS __attribute__((address_space(1)))
#define LAS __attribute__((address_space(3)))
#define DI __device__ __forceinline__
typedef unsigned short u16;
typedef unsigned long long u64;
typedef unsigned v4u __attribute__((ext_vector_type(4)));
typedef unsigned v2u __attribute__((ext_vector_type(2)));
typedef float f32x2 __attribute__((ext_vector_type(2)));
typedef float f32x4 __attribute__((ext_vector_type(4)));
typedef float f32x16 __attribute__((ext_vector_type(16)));
typedef short bf16x8 __attribute__((ext_vector_type(8)));
typedef short s16x4 __attribute__((ext_vector_type(4)));
typedef __bf16 bf2_t __attribute__((ext_vector_type(2)));

constexpr int NM = 576, NH = 32, ND = 128, NC = 512, NSK = 8192, NF = 4608;
constexpr int WS_EPOCH = 0;
constexpr int WS_HX = 4096;
constexpr int WS_QX = WS_HX + 4 * 73728;
constexpr int WS_CX = WS_QX + 262144;
constexpr int WS_QABS = WS_CX + 262144;
constexpr int WS_STAT = WS_QABS + 131072;
constexpr int WS_POUT = WS_STAT + 524288;
constexpr int WS_OPART = WS_POUT + 72 * 128 * 128;
constexpr int WS_BYTES = WS_OPART + 4194304;
constexpr int AUX_SC1 = 16;
constexpr unsigned SPIN_CAP = 1u << 17;

constexpr unsigned T_CKV = 150, T_HEADB = 480, T_MLP = 520, T_STATS = 800, T_RED = 800;
#define BULK_WIN 8
#define POLL_SLEEP 2
#define STAT_SLEEP 2
#define HO0_SLEEP 3
#define CX_SLEEP 2

DI unsigned fsw(unsigned r) { return ((r & 3u) << 2) | ((((r >> 3) ^ (r >> 2)) & 1u) << 1) | ((r >> 3) & 1u); }
DI float bflo(unsigned v) { return __uint_as_float(v << 16); }
DI float bfhi(unsigned v) { return __uint_as_float(v & 0xffff0000u); }
DI unsigned pk2(float a, float b) { f32x2 f = {a, b}; bf2_t r = __builtin_convertvector(f, bf2_t); return __builtin_bit_cast(unsigned, r); }
DI unsigned bf1(float a) { return pk2(a, 0.f) & 0xffffu; }
DI f32x4 mfma16(v4u a, v4u b, f32x4 c) { return __builtin_amdgcn_mfma_f32_16x16x32_bf16(__builtin_bit_cast(bf16x8, a), __builtin_bit_cast(bf16x8, b), c, 0, 0, 0); }
DI f32x16 mfma32(v4u a, v4u b, f32x16 c) { return __builtin_amdgcn_mfma_f32_32x32x16_bf16(__builtin_bit_cast(bf16x8, a), __builtin_bit_cast(bf16x8, b), c, 0, 0, 0); }
DI v2u trread(LAS char* p) { s16x4 r = __builtin_amdgcn_ds_read_tr16_b64_v4i16((LAS s16x4*)p); return __builtin_bit_cast(v2u, r); }
DI v4u trfrag(LAS char* p0, LAS char* p1) { v2u a = trread(p0), b = trread(p1); return v4u{a.x, a.y, b.x, b.y}; }
DI v4u lds128(LAS char* p) { return *(LAS v4u*)p; }
DI v4u gld(const u16* p) { return *(const GAS v4u*)p; }
template <int CTRL> DI float dppf(float x) { return __builtin_bit_cast(float, __builtin_amdgcn_mov_dpp(__builtin_bit_cast(int, x), CTRL, 0xf, 0xf, true)); }
template <int CTRL> DI unsigned dppu(unsigned x) { return (unsigned)__builtin_amdgcn_mov_dpp((int)x, CTRL, 0xf, 0xf, true); }
DI float sum32(float x) { auto t = __builtin_amdgcn_permlane32_swap(__float_as_uint(x), __float_as_uint(x), false, false); return __uint_as_float(t[0]) + __uint_as_float(t[1]); }
DI float max32(float x) { auto t = __builtin_amdgcn_permlane32_swap(__float_as_uint(x), __float_as_uint(x), false, false); return fmaxf(__uint_as_float(t[0]), __uint_as_float(t[1])); }
DI float sum16(float x) { auto t = __builtin_amdgcn_permlane16_swap(__float_as_uint(x), __float_as_uint(x), false, false); return __uint_as_float(t[0]) + __uint_as_float(t[1]); }
DI float max16(float x) { auto t = __builtin_amdgcn_permlane16_swap(__float_as_uint(x), __float_as_uint(x), false, false); return fmaxf(__uint_as_float(t[0]), __uint_as_float(t[1])); }
DI float wave_sum(float x) { x += dppf<0xB1>(x); x += dppf<0x4E>(x); x += dppf<0x141>(x); x += dppf<0x128>(x); x = sum16(x); return sum32(x); }
DI float wave_max(float x) { x = fmaxf(x, dppf<0xB1>(x)); x = fmaxf(x, dppf<0x4E>(x)); x = fmaxf(x, dppf<0x141>(x)); x = fmaxf(x, dppf<0x128>(x)); x = max16(x); return max32(x); }
DI void wait_until(u64 t) { while (__builtin_amdgcn_s_memrealtime() < t) __builtin_amdgcn_s_sleep(1); }
#define DRAIN() asm volatile("s_waitcnt vmcnt(0)" ::: "memory")
#define CBAR() asm volatile("" ::: "memory")
#define SCHED() __builtin_amdgcn_sched_barrier(0)
#define LBAR() asm volatile("s_waitcnt lgkmcnt(0)\n\ts_barrier" ::: "memory")
#define DMA16(gp, ldsoff) do { unsigned keep_; asm volatile("s_mov_b32 %0, m0\n\ts_mov_b32 m0, %2\n\ts_nop 0\n\tglobal_load_lds_dwordx4 %1, off nt\n\ts_mov_b32 m0, %0" : "=&s"(keep_) : "v"(gp), "s"((unsigned)(size_t)L + (unsigned)(ldsoff)) : "memory"); } while (0)
#define DMA16X(gp, ldsoff) do { unsigned keep_; asm volatile("s_mov_b32 %0, m0\n\ts_mov_b32 m0, %2\n\ts_nop 0\n\tglobal_load_lds_dwordx4 %1, off\n\ts_mov_b32 m0, %0" : "=&s"(keep_) : "v"(gp), "s"((unsigned)(size_t)L + (unsigned)(ldsoff)) : "memory"); } while (0)
#define DMA16W(gp, ldsoff) do { DMA16(gp, ldsoff); if (BULK_WIN < 32) asm volatile("s_waitcnt vmcnt(%0)" :: "i"(BULK_WIN - 1) : "memory"); } while (0)
#define LDG(off) __builtin_amdgcn_raw_buffer_load_b128(rs, (off), 0, AUX_SC1)
#define STG(v, off) __builtin_amdgcn_raw_buffer_store_b128((v), rs, (off), 0, AUX_SC1)
#define TAGOK(g) (((g).x == tagA) & ((g).z == tagB))

DI int tile_lane_off(int lane, int ld) { const int r8 = lane >> 3; return (8 * (r8 >> 2) + (r8 & 3)) * ld + 8 * ((lane & 7) ^ ((r8 >> 1) << 1)); }
DI int tile_tr_off(int lane, int ct) { const int r8 = 4 * ((lane >> 4) & 1) + ((lane >> 2) & 3); return (lane >> 5) * 1024 + r8 * 128 + ((ct ^ (r8 >> 1)) * 32) + (lane & 3) * 8; }
DI void put_hilo(LAS char* base, int stride, int d, float v0, float v1) {
  const unsigned h = pk2(v0, v1);
  const unsigned l = pk2(v0 - bflo(h), v1 - bfhi(h));
  *(LAS u16*)(base + d * 2) = (u16)h;
  *(LAS u16*)(base + stride + d * 2) = (u16)(h >> 16);
  *(LAS u16*)(base + 2 * stride + d * 2) = (u16)l;
  *(LAS u16*)(base + 3 * stride + d * 2) = (u16)(l >> 16);
}

__global__ void __launch_bounds__(512, 2)
fused_kernel(const u16* __restrict__ x, const u16* __restrict__ ckv, const u16* __restrict__ wq, const u16* __restrict__ wuk,
             const u16* __restrict__ wuv, const u16* __restrict__ wo, const u16* __restrict__ w1, const u16* __restrict__ b1,
             const u16* __restrict__ w2, const u16* __restrict__ b2, const u16* __restrict__ gw, u16* __restrict__ out, char* ws) {
  __shared__ __attribute__((aligned(16))) char smem[163840];
  LAS char* L = (LAS char*)smem;
  const int tid = threadIdx.x, lane = tid & 63, w = __builtin_amdgcn_readfirstlane(tid >> 6);
  const int bid = blockIdx.x, xcd = bid & 7, kk = bid >> 3;
  const u64 t0 = __builtin_amdgcn_s_memrealtime();
  const __amdgpu_buffer_rsrc_t rs = __builtin_amdgcn_make_buffer_rsrc((void*)ws, 0, WS_BYTES, 0x00020000);
  const int g4 = lane >> 4, col = lane & 15;

  if (kk < 16) {
    const int h = xcd * 4 + (kk >> 2), j = kk & 3;
    constexpr int WQ = 0, WUK = 40960, W1 = 69632, W2 = W1 + 40960, W2X = W2 + 36864, WUV = 0, WO = 32768;
    constexpr int RED = W2X + 9216, CTXA = RED + 4096, QA = CTXA + 1024, PRIV = RED, HR = QA + 512;
    const int ks0 = j < 2 ? 5 * j : 4 * j + 2;
    u64 epoch;
    asm volatile("global_load_dwordx2 %0, %1, off sc1" : "=&v"(epoch) : "v"(ws + WS_EPOCH) : "memory");
    constexpr int XG = W2X;
    if (w < 3) { const int c = 64 * w + lane; DMA16X(x + 8 * (c < 144 ? c : 0), XG + w * 1024); }
    else if (w < 5) { const int c = 64 * (w - 3) + lane; DMA16X(gw + 8 * (c < 72 ? c : 0), XG + w * 1024); }
    {
      const u16* src = wq + (size_t)(32 * ks0) * 4096 + h * ND + tile_lane_off(lane, 4096) + (16 * (w & 1) + 4 * (w >> 2)) * 4096 + 64 * ((w >> 1) & 1);
#pragma unroll
      for (int i = 0; i < 5; ++i)
        if (i < 4 || j < 2) DMA16(src + (size_t)i * 32 * 4096, WQ + (i * 8 + w) * 1024);
#pragma unroll
      for (int i = 0; i < 4; ++i) {
        const int row = 16 * w + 4 * i + g4;
        DMA16(wuk + (size_t)(128 * j + row) * 4096 + h * ND + 8 * (col ^ fsw(4 * i + g4)), WUK + (4 * w + i) * 1024);
      }
    }
    asm volatile("s_waitcnt vmcnt(4)\n\ts_barrier" : "+v"(epoch) :: "memory");
    v4u xa[5];
#pragma unroll
    for (int i = 0; i < 5; ++i) xa[i] = lds128(L + XG + (col & 1) * 1152 + 64 * (ks0 + ((i < 4 || j < 2) ? i : 0)) + 16 * g4);
    epoch += 1ull;
    if ((unsigned)epoch == 0u) epoch += 1ull;
    const unsigned tagA = (unsigned)epoch, tagB = (unsigned)(epoch >> 32);
    {
      f32x4 acc = {0.f, 0.f, 0.f, 0.f};
      LAS char* p = L + WQ + (w >> 2) * 2048 + tile_tr_off(lane, w & 3);
      v4u bq[5];
#pragma unroll
      for (int i = 0; i < 4; ++i) bq[i] = trfrag(p + i * 8192, p + i * 8192 + 4096);
      if (j < 2) bq[4] = trfrag(p + 4 * 8192, p + 4 * 8192 + 4096);
#pragma unroll
      for (int i = 0; i < 4; ++i) acc = mfma16(xa[i], bq[i], acc);
      if (j < 2) acc = mfma16(xa[4], bq[4], acc);
      if (g4 == 0) { const v4u gr = {tagA, __float_as_uint(acc[0]), tagB, __float_as_uint(acc[1])}; STG(gr, WS_QX + ((h * 4 + j) * 128 + 16 * w + col) * 16); }
    }
    float g0, g1;
    {
      const int gc = lane + 64 < 72 ? lane + 64 : 0;
      const v4u gq[6] = {lds128(L + XG + lane * 16), lds128(L + XG + 1152 + lane * 16), lds128(L + XG + 3072 + lane * 16),
                         lds128(L + XG + gc * 16), lds128(L + XG + 1152 + gc * 16), lds128(L + XG + 3072 + gc * 16)};
      float d0 = 0.f, d1 = 0.f, e0 = 0.f, e1 = 0.f;
#pragma unroll
      for (int e = 0; e < 4; ++e) {
        d0 += bflo(gq[0][e]) * bflo(gq[2][e]) + bfhi(gq[0][e]) * bfhi(gq[2][e]);
        d1 += bflo(gq[1][e]) * bflo(gq[2][e]) + bfhi(gq[1][e]) * bfhi(gq[2][e]);
        e0 += bflo(gq[3][e]) * bflo(gq[5][e]) + bfhi(gq[3][e]) * bfhi(gq[5][e]);
        e1 += bflo(gq[4][e]) * bflo(gq[5][e]) + bfhi(gq[4][e]) * bfhi(gq[5][e]);
      }
      if (lane < 8) { d0 += e0; d1 += e1; }
      d0 = wave_sum(d0); d1 = wave_sum(d1);
      g0 = 1.f / (1.f + __expf(-d0)); g1 = 1.f / (1.f + __expf(-d1));
      asm volatile("" : "+v"(g0), "+v"(g1));
    }
    DRAIN();
    __builtin_amdgcn_s_sleep(HO0_SLEEP);
    LAS char* pv = L + PRIV + w * 256;
    {
      v4u ga;
      const int off = WS_QX + ((h * 4 + g4) * 128 + 16 * w + col) * 16;
      for (unsigned spins = 0;; ++spins) {
        CBAR();
        ga = LDG(off);
        if (__all(TAGOK(ga)) || spins > SPIN_CAP) break;
        __builtin_amdgcn_s_sleep(1);
      }
      v4u uk[4];
#pragma unroll
      for (int ks = 0; ks < 4; ++ks) uk[ks] = lds128(L + WUK + (16 * w + col) * 256 + (((4 * ks + g4) ^ fsw(col)) << 4));
      const float q0 = sum32(sum16(__uint_as_float(ga.y))), q1 = sum32(sum16(__uint_as_float(ga.w)));
      const float qv = (g4 & 1) ? q1 : q0;
      const unsigned hi = bf1(qv);
      *(LAS u16*)(L + QA + g4 * 256 + (16 * w + col) * 2) = (u16)((g4 & 2) ? bf1(qv - bflo(hi)) : hi);
      LBAR();
      f32x4 a = {0.f, 0.f, 0.f, 0.f};
#pragma unroll
      for (int ks = 0; ks < 4; ++ks) a = mfma16(lds128(L + QA + (col & 3) * 256 + ks * 64 + g4 * 16), uk[ks], a);
      const float v = ((g4 & 1) ? (a[1] + a[3]) : (a[0] + a[2])) * (0.08838834764831845f * 1.4426950408889634f);
      const unsigned bits = bf1(v);
      const unsigned n1 = dppu<0x55>(bits), n2 = dppu<0xAA>(bits), n3 = dppu<0xFF>(bits);
      if ((lane & 3) == 0 && g4 < 2) {
        const v4u gr = {tagA, bits | (n1 << 16), tagB, n2 | (n3 << 16)};
        STG(gr, WS_QABS + ((g4 * 32 + h) * 128 + 32 * j + 4 * w + (col >> 2)) * 16);
      }
    }
    if (j >= 2) {
      const int m8 = (kk >> 2) * 2 + (j - 2);
      const bool tri = m8 >= 6;
      const int ug0 = xcd * 18 + (tri ? 12 + 3 * (m8 - 6) : 2 * m8);
      const int r2 = tri ? 3840 + 48 * (xcd * 2 + m8 - 6) : 80 * (xcd * 6 + m8);
      const int nk2 = tri ? 48 : 80;
      const int W2M = tri ? 122880 : 81920;
      constexpr int W2E = 0, HA = 160768;
      v4u xm[3][5];
#pragma unroll
      for (int un = 0; un < 3; ++un) {
        const int uq = (ug0 + un) & 3, kq = uq < 2 ? 5 * uq : 4 * uq + 2;
#pragma unroll
        for (int i = 0; i < 5; ++i) xm[un][i] = lds128(L + XG + (col & 1) * 1152 + 64 * (kq + ((i < 4 || uq < 2) ? i : 0)) + 16 * g4);
      }
      LBAR();
      wait_until(t0 + T_MLP);
      unsigned b1v;
      const int hf_ = 16 * w + col;
      asm volatile("global_load_ushort %0, %1, off" : "=&v"(b1v) : "v"(b1 + r2 + (hf_ < nk2 ? hf_ : 0)) : "memory");
#pragma unroll
      for (int un = 0; un < 3; ++un) {
        if (un < 2 || tri) {
          const int uq = (ug0 + un) & 3, kq = uq < 2 ? 5 * uq : 4 * uq + 2;
          const u16* s1 = w1 + 128 * ((ug0 + un) >> 2) + tile_lane_off(lane, NF) + (size_t)(32 * kq + 16 * (w & 1) + 4 * (w >> 2)) * NF + 64 * ((w >> 1) & 1);
#pragma unroll
          for (int i = 0; i < 5; ++i)
            if (i < 4 || uq < 2) DMA16W(s1 + (size_t)i * 32 * NF, un * 40960 + (i * 8 + w) * 1024);
        }
      }
      asm volatile("s_waitcnt vmcnt(0)\n\ts_barrier" : "+v"(b1v) :: "memory");
#pragma unroll
      for (int un = 0; un < 3; ++un) {
        if (un < 2 || tri) {
          const int uq = (ug0 + un) & 3;
          LAS char* p = L + un * 40960 + (w >> 2) * 2048 + tile_tr_off(lane, w & 3);
          f32x4 acc = {0.f, 0.f, 0.f, 0.f};
#pragma unroll
          for (int i = 0; i < 4; ++i) acc = mfma16(xm[un][i], trfrag(p + i * 8192, p + i * 8192 + 4096), acc);
          if (uq < 2) acc = mfma16(xm[un][4], trfrag(p + 4 * 8192, p + 4 * 8192 + 4096), acc);
          if (g4 == 0) {
            const v4u gr = {tagA, __float_as_uint(acc[0]), tagB, __float_as_uint(acc[1])};
            STG(gr, WS_HX + (uq * NF + 128 * ((ug0 + un) >> 2) + 16 * w + col) * 16);
          }
        }
      }
      LBAR();
      if (tri) {
        const u16* s2 = w2 + (size_t)r2 * NM + tile_lane_off(lane, NM) + (16 * (w & 1) + 4 * ((w >> 1) & 1)) * NM + 64 * (w >> 2);
#pragma unroll
        for (int i = 0; i < 5; ++i)
          if (i < 4 || w < 4) DMA16W(s2 + 128 * i, 122880 + (i * 8 + w) * 1024);
      } else {
        const u16* s2 = w2 + (size_t)r2 * NM + tile_lane_off(lane, NM) + (32 * (w >> 2) + 16 * (w & 1) + 4 * ((w >> 1) & 1)) * NM;
#pragma unroll
        for (int i = 0; i < 9; ++i) DMA16W(s2 + 64 * i, 81920 + (i * 8 + w) * 1024);
      }
      {
        const u16* s3 = w2 + (size_t)(r2 + nk2 - 16) * NM + tile_lane_off(lane, NM) + 4 * (w & 1) * NM + 64 * (w >> 1);
#pragma unroll
        for (int i = 0; i < 3; ++i)
          if (i < 2 || w < 2) DMA16(s3 + 256 * i, W2E + (i * 8 + w) * 1024);
      }
      {
        const bool live = hf_ < nk2;
        const int ho = WS_HX + (g4 * NF + r2 + (live ? hf_ : 0)) * 16;
        float hv = 0.f;
        if (16 * w < nk2) {
          v4u hg;
          for (unsigned spins = 0;; ++spins) {
            CBAR();
            hg = LDG(ho);
            if (__all(TAGOK(hg)) || spins > SPIN_CAP) break;
            __builtin_amdgcn_s_sleep(4);
          }
          const float bb = bflo(b1v);
          const float p0 = fmaxf(sum32(sum16(__uint_as_float(hg.y))) + bb, 0.f), p1 = fmaxf(sum32(sum16(__uint_as_float(hg.w))) + bb, 0.f);
          hv = (g4 & 1) ? (1.f - g1) * p1 * p1 : (1.f - g0) * p0 * p0;
        }
        const unsigned hi = bf1(hv);
        const int kt_ = tri ? (w == 2 ? 4 : (w == 3 || w == 4) ? w - 1 : w) : w;
        if (w < 6) *(LAS u16*)(L + HA + g4 * 192 + (16 * kt_ + col) * 2) = (u16)((g4 & 2) ? bf1(hv - bflo(hi)) : hi);
      }
      asm volatile("s_waitcnt vmcnt(0) lgkmcnt(0)\n\ts_barrier" ::: "memory");
      {
        LAS char* ha = L + HA + (col & 3) * 192 + g4 * 16;
        const v4u h0 = lds128(ha), h1 = lds128(ha + 64), h2 = lds128(ha + 128);
        const int tro = tile_tr_off(lane, w & 3);
        f32x4 acc[5];
#pragma unroll
        for (int i = 0; i < 5; ++i) {
          acc[i] = f32x4{0.f, 0.f, 0.f, 0.f};
          if (i < 4 || w < 4) {
            LAS char* pe = L + W2E + ((w >> 2) + 2 * i) * 2048 + (tro & 1023);
            acc[i] = mfma16(h2, trfrag(pe, pe + 1024), acc[i]);
            if (tri) {
              LAS char* pm = L + W2M + ((w >> 2) + 2 * i) * 4096 + tro;
              acc[i] = mfma16(h0, trfrag(pm, pm + 2048), acc[i]);
            } else {
              LAS char* pm = L + W2M + ((w >> 2) + 2 * i) * 8192 + tro;
              acc[i] = mfma16(h0, trfrag(pm, pm + 2048), acc[i]);
              acc[i] = mfma16(h1, trfrag(pm + 4096, pm + 6144), acc[i]);
            }
          }
        }
        const f32x4 a = g4 == 0 ? acc[0] : g4 == 1 ? acc[1] : g4 == 2 ? acc[2] : acc[3];
        const int pidx = 64 + xcd * 8 + m8;
        {
          const int ct = w + 8 * g4;
          const v4u gr = {tagA, __float_as_uint(a[0] + a[2]), tagB, __float_as_uint(a[1] + a[3])};
          STG(gr, WS_POUT + ((2 * ct + (col >> 3)) * 128 + pidx) * 128 + (col & 7) * 16);
        }
        if (w < 4 && g4 == 0) {
          const v4u gr = {tagA, __float_as_uint(acc[4][0] + acc[4][2]), tagB, __float_as_uint(acc[4][1] + acc[4][3])};
          STG(gr, WS_POUT + ((2 * (w + 32) + (col >> 3)) * 128 + pidx) * 128 + (col & 7) * 16);
        }
      }
      __builtin_amdgcn_endpgm();
    }
    constexpr int BWO = 65536, BRED = 139264, BCTX = 147456, BHR = BCTX + 2048;
    {
      LBAR();
      wait_until(t0 + T_HEADB);
      const u16* src = wuv + (size_t)(256 * j) * 4096 + h * ND + tile_lane_off(lane, 4096) + (16 * (w & 1) + 4 * (w >> 2)) * 4096 + 64 * ((w >> 1) & 1);
#pragma unroll
      for (int i = 0; i < 8; ++i) DMA16(src + (size_t)i * 32 * 4096, (i * 8 + w) * 1024);
      const u16* so = wo + (size_t)(h * ND + 64 * j) * NM + tile_lane_off(lane, NM) + (32 * (w >> 2) + 16 * (w & 1) + 4 * ((w >> 1) & 1)) * NM;
#pragma unroll
      for (int i = 0; i < 9; ++i) DMA16(so + 64 * i, BWO + (i * 8 + w) * 1024);
      asm volatile("s_waitcnt vmcnt(0)\n\ts_barrier" ::: "memory");
    }
    v4u uv[8], wf[5][2];
    {
      LAS char* p = L + (w >> 2) * 2048 + tile_tr_off(lane, w & 3);
#pragma unroll
      for (int i = 0; i < 8; ++i) uv[i] = trfrag(p + i * 8192, p + i * 8192 + 4096);
      LAS char* po = L + BWO + (w >> 2) * 8192 + tile_tr_off(lane, w & 3);
#pragma unroll
      for (int i = 0; i < 5; ++i) {
        const int ii = (i < 4 || w < 4) ? i : 0;
        wf[i][0] = trfrag(po + ii * 16384, po + ii * 16384 + 2048);
        wf[i][1] = trfrag(po + ii * 16384 + 4096, po + ii * 16384 + 6144);
      }
    }
    const int b = w >> 2, q4 = w & 3;
    {
      const int off = WS_STAT + ((h * 2 + b) * 64 + 16 * q4 + (lane >> 2)) * 16;
      const int ooff = WS_OPART + ((((h * 2 + j) * 2 + b) * 64 + 16 * q4) * 512) + lane * 16;
      v4u g, o[8];
      bool ok = false;
      unsigned done = 0;
      if (w < 2) { const v4u cxw = LDG(WS_CX + ((h * 2 + j) * 128 + w * 64 + lane) * 16); asm volatile("" :: "v"(cxw)); }
      wait_until(t0 + T_STATS);
      for (unsigned spins = 0;; ++spins) {
        CBAR();
        if (!ok) { g = LDG(off); ok = TAGOK(g); }
        const u64 m = __builtin_amdgcn_ballot_w64(ok) | (spins > SPIN_CAP ? ~0ull : 0ull);
        __builtin_amdgcn_fence(__ATOMIC_ACQUIRE, "wavefront");
#pragma unroll
        for (int i = 0; i < 8; ++i)
          if (!(done & (1u << i)) && ((m >> (8 * i)) & 0xffull) == 0xffull) { o[i] = LDG(ooff + i * 1024); done |= 1u << i; }
        if (done == 255u) break;
        __builtin_amdgcn_s_sleep(STAT_SLEEP);
      }
      const float ms = __uint_as_float(g.y), ls = (lane & 3) == 0 ? __uint_as_float(g.w) : 0.f;
      float Mw = fmaxf(ms, dppf<0x141>(ms));
      Mw = fmaxf(Mw, dppf<0x128>(Mw)); Mw = max32(max16(Mw));
      const float wgt = __builtin_amdgcn_exp2f(ms - Mw);
      float ww[8];
#pragma unroll
      for (int i = 0; i < 8; ++i) ww[i] = __uint_as_float((unsigned)__builtin_amdgcn_ds_bpermute((2 * i + (lane >> 5)) * 16, (int)__float_as_uint(wgt)));
      const float Lw = wave_sum(wgt * ls);
      if (lane == 0) *(LAS f32x2*)(L + BHR + w * 8) = f32x2{Mw, Lw};
      asm volatile("" : "+v"(ww[0]), "+v"(ww[1]), "+v"(ww[2]), "+v"(ww[3]), "+v"(ww[4]), "+v"(ww[5]), "+v"(ww[6]), "+v"(ww[7]));
      SCHED();
      float acc[8] = {0.f, 0.f, 0.f, 0.f, 0.f, 0.f, 0.f, 0.f};
#pragma unroll
      for (int i = 0; i < 8; ++i) {
        acc[0] += ww[i] * bflo(o[i].x); acc[1] += ww[i] * bfhi(o[i].x);
        acc[2] += ww[i] * bflo(o[i].y); acc[3] += ww[i] * bfhi(o[i].y);
        acc[4] += ww[i] * bflo(o[i].z); acc[5] += ww[i] * bfhi(o[i].z);
        acc[6] += ww[i] * bflo(o[i].w); acc[7] += ww[i] * bfhi(o[i].w);
      }
      f32x4 r4;
#pragma unroll
      for (int e = 0; e < 4; ++e) {
        auto t = __builtin_amdgcn_permlane32_swap(__float_as_uint(acc[e]), __float_as_uint(acc[4 + e]), false, false);
        r4[e] = __uint_as_float(t[0]) + __uint_as_float(t[1]);
      }
      *(LAS f32x4*)(L + BRED + w * 1024 + (lane & 31) * 32 + (lane >> 5) * 16) = r4;
    }
    LBAR();
    {
      const int c = tid & 255;
      LAS float* r = (LAS float*)(L + BRED + b * 4096) + c;
      const f32x4 s0 = *(LAS f32x4*)(L + BHR + b * 32), s1 = *(LAS f32x4*)(L + BHR + b * 32 + 16);
      const float r0 = r[0], r1 = r[256], r2_ = r[512], r3 = r[768];
      const float Ma = fmaxf(fmaxf(s0[0], s0[2]), fmaxf(s1[0], s1[2]));
      const float a0 = __builtin_amdgcn_exp2f(s0[0] - Ma), a1 = __builtin_amdgcn_exp2f(s0[2] - Ma), a2 = __builtin_amdgcn_exp2f(s1[0] - Ma), a3 = __builtin_amdgcn_exp2f(s1[2] - Ma);
      const float ia = __builtin_amdgcn_rcpf((a0 * s0[1] + a1 * s0[3]) + (a2 * s1[1] + a3 * s1[3]));
      const float v = ((a0 * r0 + a1 * r1) + (a2 * r2_ + a3 * r3)) * ia;
      const unsigned hi = bf1(v);
      *(LAS u16*)(L + BCTX + b * 512 + c * 2) = (u16)hi;
      *(LAS u16*)(L + BCTX + (2 + b) * 512 + c * 2) = (u16)bf1(v - bflo(hi));
    }
    LBAR();
    {
      v4u af[8];
#pragma unroll
      for (int i = 0; i < 8; ++i) af[i] = lds128(L + BCTX + (col & 3) * 512 + i * 64 + g4 * 16);
      SCHED();
      f32x4 acc = {0.f, 0.f, 0.f, 0.f}, acc2 = {0.f, 0.f, 0.f, 0.f};
#pragma unroll
      for (int i = 0; i < 4; ++i) { acc = mfma16(af[2 * i], uv[2 * i], acc); acc2 = mfma16(af[2 * i + 1], uv[2 * i + 1], acc2); }
      acc += acc2;
      if (g4 == 0) {
        const v4u gr = {tagA, __float_as_uint(acc[0] + acc[2]), tagB, __float_as_uint(acc[1] + acc[3])};
        STG(gr, WS_CX + (((h * 2 + (w >> 2)) * 2 + j) * 64 + 16 * (w & 3) + col) * 16);
      }
    }
    __builtin_amdgcn_s_sleep(CX_SLEEP);
    {
      v4u ga, gb;
      const int off = WS_CX + ((h * 2 + j) * 128 + lane) * 16;
      for (unsigned spins = 0;; ++spins) {
        CBAR();
        ga = LDG(off); gb = LDG(off + 1024);
        if (__all(TAGOK(ga) & TAGOK(gb)) || spins > SPIN_CAP) break;
        __builtin_amdgcn_s_sleep(1);
      }
      LAS char* pb = L + BRED + w * 512;
      put_hilo(pb, 128, lane, g0 * (__uint_as_float(ga.y) + __uint_as_float(gb.y)), g1 * (__uint_as_float(ga.w) + __uint_as_float(gb.w)));
      const v4u ca0 = lds128(pb + (col & 3) * 128 + g4 * 16), ca1 = lds128(pb + (col & 3) * 128 + 64 + g4 * 16);
      f32x4 acc[5];
#pragma unroll
      for (int i = 0; i < 5; ++i) acc[i] = mfma16(ca1, wf[i][1], mfma16(ca0, wf[i][0], f32x4{0.f, 0.f, 0.f, 0.f}));
      const f32x4 a = g4 == 0 ? acc[0] : g4 == 1 ? acc[1] : g4 == 2 ? acc[2] : acc[3];
      const int pidx = h * 2 + j;
      {
        const int ct = w + 8 * g4;
        const v4u gr = {tagA, __float_as_uint(a[0] + a[2]), tagB, __float_as_uint(a[1] + a[3])};
        STG(gr, WS_POUT + ((2 * ct + (col >> 3)) * 128 + pidx) * 128 + (col & 7) * 16);
      }
      if (w < 4 && g4 == 0) {
        const v4u gr = {tagA, __float_as_uint(acc[4][0] + acc[4][2]), tagB, __float_as_uint(acc[4][1] + acc[4][3])};
        STG(gr, WS_POUT + ((2 * (w + 32) + (col >> 3)) * 128 + pidx) * 128 + (col & 7) * 16);
      }
    }
    __builtin_amdgcn_endpgm();
  } else {
    const int a = xcd * 16 + (kk - 16), b = a >> 6, s = a & 63;
    constexpr int QO = 131072, XCH = 32768, PB = XCH + 16384, MX = XCH + 24576, LS = XCH + 25600;
    u64 epoch;
    asm volatile("global_load_dwordx2 %0, %1, off sc1" : "=&v"(epoch) : "v"(ws + WS_EPOCH) : "memory");
    {
      const u16* src = ckv + ((size_t)b * NSK + 128 * s + 16 * w) * NC;
      wait_until(t0 + T_CKV);
#pragma unroll
      for (int i = 0; i < 16; ++i) DMA16(src + i * NC + ((lane ^ fsw(i)) * 8), (16 * w + i) * 1024);
    }
    const int r32 = lane & 31, hf = lane >> 5, kt = w & 3, ch = w >> 2;
    const unsigned fr = fsw(r32 & 15);
    const unsigned ob = r32 * 1024 + ch * 512 + ((hf ^ fr) << 4);
    asm volatile("s_waitcnt vmcnt(0)\n\ts_barrier" : "+v"(epoch) :: "memory");
    epoch += 1ull;
    if ((unsigned)epoch == 0u) epoch += 1ull;
    const unsigned tagA = (unsigned)epoch, tagB = (unsigned)(epoch >> 32);
    v4u kf[16], vf[8][2];
    {
      const int gl = (w & 1) * 64 + lane, hw = w >> 1;
      const int off = WS_QABS + ((b * 32 + hw) * 128 + gl) * 16;
      v4u g[8];
#pragma unroll
      for (int u = 0; u < 8; ++u) g[u] = LDG(off + u * 8192);
      SCHED();
#pragma unroll
      for (int st = 0; st < 16; ++st) kf[st] = lds128(L + kt * 32768 + ((ob ^ ((st & 7) << 5)) + (st >> 3) * 256));
      const int blk = (lane >> 4) & 1, q = (lane >> 2) & 3, p = lane & 3;
      unsigned va[2][2];
#pragma unroll
      for (int t = 0; t < 2; ++t)
#pragma unroll
        for (int cc = 0; cc < 2; ++cc)
          va[t][cc] = (8 * hf + 4 * t + q) * 1024 + (((8 * w + 4 * cc + 2 * blk + (p >> 1)) ^ (4 * q + 2 * (hf ^ t) + hf)) << 4) + 8 * (p & 1);
#pragma unroll
      for (int ks = 0; ks < 8; ++ks)
#pragma unroll
        for (int cc = 0; cc < 2; ++cc) vf[ks][cc] = trfrag(L + va[0][cc] + ks * 16384, L + va[1][cc] + ks * 16384);
      SCHED();
      unsigned todo = 0;
#pragma unroll
      for (int u = 0; u < 8; ++u) if (!__all(TAGOK(g[u]))) todo |= 1u << u;
      for (unsigned spins = 0; todo != 0 && spins < SPIN_CAP; ++spins) {
        __builtin_amdgcn_s_sleep(POLL_SLEEP);
        CBAR();
#pragma unroll
        for (int u = 0; u < 8; ++u) if (todo & (1u << u)) g[u] = LDG(off + u * 8192);
#pragma unroll
        for (int u = 0; u < 8; ++u) if (todo & (1u << u)) { if (__all(TAGOK(g[u]))) todo &= ~(1u << u); }
      }
      const unsigned qb = QO + hw * 1024 + (((gl >> 1) ^ (hw << 2)) << 4) + (gl & 1) * 8;
#pragma unroll
      for (int u = 0; u < 8; ++u) *(LAS v2u*)(L + ((qb ^ (fsw((4 * u) & 15) << 4)) + u * 4096)) = v2u{g[u].y, g[u].w};
    }
    {
      const int go = WS_OPART + (((((lane >> 3) * 2 + (w >> 2)) * 2 + b) * 64 + s) * 512) + (w & 3) * 128 + ((lane & 7) << 4);
      const v4u z = {0u, 0u, 0u, 0u};
#pragma unroll
      for (int i = 0; i < 4; ++i) STG(z, go + i * (8 * 4 * 2 * 64 * 256));
    }
    LBAR();
    f32x16 S;
#pragma unroll
    for (int i = 0; i < 16; ++i) S[i] = 0.f;
    {
      v4u qf[16];
#pragma unroll
      for (int st = 0; st < 16; ++st) qf[st] = lds128(L + QO + ((ob ^ ((st & 7) << 5)) + (st >> 3) * 256));
      SCHED();
#pragma unroll
      for (int st = 0; st < 16; ++st) S = mfma32(kf[st], qf[st], S);
    }
    float sv[8];
    if (ch == 0) {
      LAS f32x4* xw = (LAS f32x4*)(L + XCH + w * 2048 + lane * 16);
      xw[0] = f32x4{S[8], S[9], S[10], S[11]}; xw[64] = f32x4{S[12], S[13], S[14], S[15]};
#pragma unroll
      for (int e = 0; e < 8; ++e) sv[e] = S[e];
    } else {
      LAS f32x4* xw = (LAS f32x4*)(L + XCH + w * 2048 + lane * 16);
      xw[0] = f32x4{S[0], S[1], S[2], S[3]}; xw[64] = f32x4{S[4], S[5], S[6], S[7]};
#pragma unroll
      for (int e = 0; e < 8; ++e) sv[e] = S[8 + e];
    }
    LBAR();
    {
      LAS f32x4* xr = (LAS f32x4*)(L + XCH + (w ^ 4) * 2048 + lane * 16);
      const f32x4 p0 = xr[0], p1 = xr[64];
#pragma unroll
      for (int e = 0; e < 4; ++e) { sv[e] += p0[e]; sv[4 + e] += p1[e]; }
      float m = fmaxf(fmaxf(fmaxf(sv[0], sv[1]), fmaxf(sv[2], sv[3])), fmaxf(fmaxf(sv[4], sv[5]), fmaxf(sv[6], sv[7])));
      m = max32(m);
      *(LAS float*)(L + MX + r32 * 32 + w * 4) = m;
    }
    LBAR();
    float Mx;
    {
      const f32x4 m0 = *(LAS f32x4*)(L + MX + r32 * 32), m1 = *(LAS f32x4*)(L + MX + r32 * 32 + 16);
      Mx = fmaxf(fmaxf(fmaxf(m0[0], m0[1]), fmaxf(m0[2], m0[3])), fmaxf(fmaxf(m1[0], m1[1]), fmaxf(m1[2], m1[3])));
      float l = 0.f;
#pragma unroll
      for (int e = 0; e < 8; ++e) { sv[e] = __builtin_amdgcn_exp2f(sv[e] - Mx); l += sv[e]; }
#pragma unroll
      for (int i = 0; i < 2; ++i)
        *(LAS v2u*)(L + PB + r32 * 256 + (((4 * kt + 2 * ch + i) ^ fr) << 4) + hf * 8) = v2u{pk2(sv[4 * i], sv[4 * i + 1]), pk2(sv[4 * i + 2], sv[4 * i + 3])};
      l = sum32(l);
      *(LAS float*)(L + LS + r32 * 32 + w * 4) = l;
    }
    LBAR();
    float Ls;
    f32x16 O0, O1;
#pragma unroll
    for (int i = 0; i < 16; ++i) { O0[i] = 0.f; O1[i] = 0.f; }
    {
      const unsigned pb = PB + r32 * 256 + ((hf ^ fr) << 4);
      v4u pf[8];
#pragma unroll
      for (int ks = 0; ks < 8; ++ks) pf[ks] = lds128(L + (pb ^ (ks << 5)));
      const f32x4 l0 = *(LAS f32x4*)(L + LS + r32 * 32), l1 = *(LAS f32x4*)(L + LS + r32 * 32 + 16);
      SCHED();
#pragma unroll
      for (int ks = 0; ks < 8; ++ks) {
        O0 = mfma32(vf[ks][0], pf[ks], O0);
        O1 = mfma32(vf[ks][1], pf[ks], O1);
      }
      Ls = ((l0[0] + l0[1]) + (l0[2] + l0[3])) + ((l1[0] + l1[1]) + (l1[2] + l1[3]));
    }
    {
      LAS char* ot = L + w * 4096;
      const unsigned ow = r32 * 128 + 8 * hf;
#pragma unroll
      for (int gq = 0; gq < 4; ++gq) {
        *(LAS v2u*)(ot + ow + ((gq ^ (r32 & 7)) << 4)) = v2u{pk2(O0[4 * gq], O0[4 * gq + 1]), pk2(O0[4 * gq + 2], O0[4 * gq + 3])};
        *(LAS v2u*)(ot + ow + (((4 + gq) ^ (r32 & 7)) << 4)) = v2u{pk2(O1[4 * gq], O1[4 * gq + 1]), pk2(O1[4 * gq + 2], O1[4 * gq + 3])};
      }
      DRAIN();
      const int go = WS_OPART + (((((lane >> 3) * 2 + (w >> 2)) * 2 + b) * 64 + s) * 512) + (w & 3) * 128 + (((lane & 7) ^ ((lane >> 3) & 7)) << 4);
#pragma unroll
      for (int i = 0; i < 4; ++i) __builtin_amdgcn_raw_buffer_store_b128(lds128(ot + i * 1024 + lane * 16), rs, go + i * (8 * 4 * 2 * 64 * 256), 0, AUX_SC1);
    }
    SCHED();
    asm volatile("s_waitcnt vmcnt(0)\n\ts_barrier" ::: "memory");
    if (w == 0 && lane < 32) {
      const v4u gr = {tagA, __float_as_uint(Mx), tagB, __float_as_uint(Ls)};
      STG(gr, WS_STAT + ((lane * 2 + b) * 64 + s) * 16);
    }
    if ((a & 15) < 9) {
      const int r = (a >> 4) * 9 + (a & 15), t2 = tid - 256;
      float g0 = 0.f, g1 = 0.f, bias2 = 0.f;
      if (w == 4) {
        bias2 = bflo((unsigned)b2[8 * r + (lane & 7)]);
        float d0 = 0.f, d1 = 0.f;
#pragma unroll
        for (int i = 0; i < 2; ++i) {
          const int c = lane + 64 * i;
          if (c < 72) {
            const v4u xa = gld(x + c * 8), xb = gld(x + NM + c * 8), gg = gld(gw + c * 8);
#pragma unroll
            for (int e = 0; e < 4; ++e) {
              d0 += bflo(xa[e]) * bflo(gg[e]) + bfhi(xa[e]) * bfhi(gg[e]);
              d1 += bflo(xb[e]) * bflo(gg[e]) + bfhi(xb[e]) * bfhi(gg[e]);
            }
          }
        }
        d0 = wave_sum(d0); d1 = wave_sum(d1);
        g0 = 1.f / (1.f + __expf(-d0)); g1 = 1.f / (1.f + __expf(-d1));
      }
      const int off = WS_POUT + r * 16384 + tid * 16;
      v4u ga[2];
      wait_until(t0 + T_RED);
      for (unsigned spins = 0;; ++spins) {
        CBAR();
#pragma unroll
        for (int i = 0; i < 2; ++i) ga[i] = LDG(off + i * 8192);
        if (__all(TAGOK(ga[0]) & TAGOK(ga[1])) || spins > SPIN_CAP) break;
        __builtin_amdgcn_s_sleep(POLL_SLEEP);
      }
      float v0 = __uint_as_float(ga[0].y) + __uint_as_float(ga[1].y);
      float v1 = __uint_as_float(ga[0].w) + __uint_as_float(ga[1].w);
      v0 += dppf<0x128>(v0); v1 += dppf<0x128>(v1);
      v0 = sum32(sum16(v0)); v1 = sum32(sum16(v1));
      if (lane < 8) { *(LAS float*)(L + 65536 + (lane * 8 + w) * 4) = v0; *(LAS float*)(L + 65536 + 256 + (lane * 8 + w) * 4) = v1; }
      LBAR();
      if (t2 >= 0 && t2 < 16) {
        const f32x4 p0 = *(LAS f32x4*)(L + 65536 + t2 * 32), p1 = *(LAS f32x4*)(L + 65536 + t2 * 32 + 16);
        const float v = ((p0[0] + p0[1]) + (p0[2] + p0[3])) + ((p1[0] + p1[1]) + (p1[2] + p1[3])) + (1.f - (t2 < 8 ? g0 : g1)) * bias2;
        out[(t2 >> 3) * NM + 8 * r + (t2 & 7)] = (u16)bf1(v);
      }
      if (r == 0 && tid == 0) __hip_atomic_store((GAS u64*)(ws + WS_EPOCH), epoch, __ATOMIC_RELAXED, __HIP_MEMORY_SCOPE_AGENT);
    }
  }
}

extern "C" void kernel_launch(void* const* d_in, const int* in_sizes, int n_in, void* d_out, int out_size, void* d_ws, size_t ws_size,
                              hipStream_t stream) {
  fused_kernel<<<256, 512, 0, stream>>>((const u16*)d_in[0], (const u16*)d_in[1], (const u16*)d_in[2], (const u16*)d_in[3],
                                        (const u16*)d_in[4], (const u16*)d_in[5], (const u16*)d_in[7], (const u16*)d_in[8],
                                        (const u16*)d_in[9], (const u16*)d_in[10], (const u16*)d_in[11], (u16*)d_out, (char*)d_ws);
}
```
